```python
import torch

def _softmax(x, axis=-1):
    return torch.softmax(x, dim=axis)
def _gelu(x):
    return torch.nn.functional.gelu(x, approximate="tanh")
def _silu(x):
    return torch.nn.functional.silu(x)
def _sigmoid(x):
    return torch.sigmoid(x)
def _layernorm(x, axis=-1, eps=1e-5):
    m = x.mean(dim=axis, keepdim=True)
    v = x.var(dim=axis, keepdim=True, unbiased=False)
    return (x - m) / torch.sqrt(v + eps)
def _scatter_add(tgt, idx, src, axis=0):
    return tgt.clone().index_add_(axis, idx, src)
def _segment_sum(src, seg, n):
    out = torch.zeros((n,) + src.shape[1:], dtype=src.dtype)
    return out.index_add_(0, seg, src)
def _pad(x, pw):
    flat = tuple(v for pair in reversed(pw) for v in pair)
    return torch.nn.functional.pad(x, flat)

kernel_name = 'parallel_sum__mha_block__dense_activation_relu__prompt'

def setup_inputs():

    _w = (lambda a: (lambda t: t.to(torch.bfloat16) if t.is_floating_point() else t)(torch.as_tensor(a)))
    def _A_setup():
        B, S, D, H = 1, 2048, 1024, 32
        return {
            "x": _w((lambda *s: torch.randn(*s, dtype=torch.bfloat16) * 0.05)(B, S, D)),
            "wqkv": _w((lambda *s: torch.randn(*s, dtype=torch.bfloat16) * 0.05)(D, 3 * D)),
            "wo": _w((lambda *s: torch.randn(*s, dtype=torch.bfloat16) * 0.05)(D, D)),
        }
    _A_in = _A_setup()

    def _B_setup():
        B, S, D, Dff = 1, 2048, 1024, 3584
        return {
            "x": _w((lambda *s: torch.randn(*s, dtype=torch.bfloat16) * 0.05)(B, S, D)),
            "w1": _w((lambda *s: torch.randn(*s, dtype=torch.bfloat16) * 0.05)(D, Dff)),
            "w2": _w((lambda *s: torch.randn(*s, dtype=torch.bfloat16) * 0.05)(Dff, D)),
        }
    _B_in = _B_setup()

    return {
        "A_x": _A_in["x"],
        "A_wqkv": _A_in["wqkv"],
        "A_wo": _A_in["wo"],
        "B_x": _B_in["x"],
        "B_w1": _B_in["w1"],
        "B_w2": _B_in["w2"],
    }

def reference(A_x, A_wqkv, A_wo, B_x, B_w1, B_w2):
    def _A_ref(x, wqkv, wo):
        B, S, D = x.shape
        H = 32
        Dh = D // H
        qkv = (lambda x, *d: x.permute(*d))((x @ wqkv).reshape(B, S, 3, H, Dh), 2, 0, 3, 1, 4)
        q, k, v = qkv[0], qkv[1], qkv[2]
        scores = torch.einsum("bhqd,bhkd->bhqk", q, k) * (Dh ** -0.5)
        p = _softmax(scores, axis=-1)
        o = torch.einsum("bhqk,bhkd->bhqd", p, v)
        return (lambda x, *d: x.permute(*d))(o, 0, 2, 1, 3).reshape(B, S, D) @ wo

    def _B_ref(x, w1, w2):
        h = torch.einsum("bsd,df->bsf", x, w1)
        zero = torch.zeros_like(h)
        h = torch.maximum(h, zero)
        return torch.einsum("bsf,fd->bsd", h, w2)

    x = A_x
    xn = _layernorm(x, axis=-1)
    return x + _A_ref(xn, A_wqkv, A_wo) + _B_ref(xn, B_w1, B_w2)
```

```cpp
#include <hip/hip_runtime.h>
#include <hip/hip_bf16.h>
#include <stdint.h>

typedef unsigned short u16;
using bf16x8 = __attribute__((ext_vector_type(8))) short;
using s16x4  = __attribute__((ext_vector_type(4))) short;
using f32x4  = __attribute__((ext_vector_type(4))) float;
using f32x2  = __attribute__((ext_vector_type(2))) float;
using f32x16 = __attribute__((ext_vector_type(16))) float;
using u32x4  = __attribute__((ext_vector_type(4))) unsigned;
using u32x2  = __attribute__((ext_vector_type(2))) unsigned;
typedef __bf16 bf16x2_t __attribute__((ext_vector_type(2)));
typedef __attribute__((address_space(3))) const char* lds_cptr;
typedef __attribute__((address_space(3))) const bf16x8* lds_b128;
typedef __attribute__((address_space(3))) s16x4* lds_tr;
#define DI __device__ __forceinline__

constexpr int S_ = 2048, D_ = 1024, H_ = 32, DFF = 3584, NQKV = 3072;
constexpr size_t WS_XN = 0, WS_Q = 4u << 20, WS_K = 8u << 20, WS_V = 12u << 20, WS_A2 = 16u << 20, WS_P = 40u << 20;
constexpr int KB2 = 288;

DI unsigned pk_bf16(float lo, float hi) {
    bf16x2_t r = __builtin_convertvector((f32x2){lo, hi}, bf16x2_t);
    return __builtin_bit_cast(unsigned, r);
}
DI float bf2f(u16 v) { return __uint_as_float((unsigned)v << 16); }
DI float rbf(float v) { return __uint_as_float(pk_bf16(v, 0.f) << 16); }

DI void glds16(const void* g, unsigned lds_base) {
    unsigned sv;
    asm volatile("s_mov_b32 %0, m0\n\ts_mov_b32 m0, %2\n\ts_nop 0\n\tglobal_load_lds_dwordx4 %1, off\n\ts_mov_b32 m0, %0"
                 : "=&s"(sv) : "v"(g), "s"(lds_base) : "memory");
}
DI void glds16x2(const void* g, unsigned lds_base) {
    unsigned sv;
    asm volatile("s_mov_b32 %0, m0\n\ts_mov_b32 m0, %2\n\ts_nop 0\n\tglobal_load_lds_dwordx4 %1, off\n\t"
                 "global_load_lds_dwordx4 %1, off offset:1024\n\ts_mov_b32 m0, %0"
                 : "=&s"(sv) : "v"(g), "s"(lds_base) : "memory");
}
DI s16x4 tr_read(lds_cptr p) { return __builtin_amdgcn_ds_read_tr16_b64_v4i16((lds_tr)p); }
DI bf16x8 cat8(s16x4 lo, s16x4 hi) { return __builtin_shufflevector(lo, hi, 0, 1, 2, 3, 4, 5, 6, 7); }
#define MFMA16(a, b, c) __builtin_amdgcn_mfma_f32_16x16x32_bf16((a), (b), (c), 0, 0, 0)
#define MFMA32(a, b, c) __builtin_amdgcn_mfma_f32_32x32x16_bf16((a), (b), (c), 0, 0, 0)
#define WAITBAR(N) asm volatile("s_waitcnt vmcnt(" #N ") lgkmcnt(0)\n\ts_barrier" ::: "memory")

__global__ __launch_bounds__(256) void ln_kernel(const u16* __restrict__ x, u16* __restrict__ xn) {
    const int lane = threadIdx.x & 63, wid = threadIdx.x >> 6;
#pragma unroll
    for (int rr = 0; rr < 2; ++rr) {
        const int m = blockIdx.x * 8 + wid * 2 + rr;
        const u32x4* src = (const u32x4*)(x + (size_t)m * D_);
        u32x4 raw[2] = {src[lane], src[lane + 64]};
        float v[16];
#pragma unroll
        for (int c = 0; c < 2; ++c)
#pragma unroll
            for (int i = 0; i < 4; ++i) {
                v[c * 8 + 2 * i] = __uint_as_float(raw[c][i] << 16);
                v[c * 8 + 2 * i + 1] = __uint_as_float(raw[c][i] & 0xffff0000u);
            }
        float s = 0.f;
#pragma unroll
        for (int i = 0; i < 16; ++i) s += v[i];
#pragma unroll
        for (int o = 1; o < 64; o <<= 1) s += __shfl_xor(s, o);
        const float mean = s * (1.f / D_);
        float q = 0.f;
#pragma unroll
        for (int i = 0; i < 16; ++i) { const float d = v[i] - mean; q += d * d; }
#pragma unroll
        for (int o = 1; o < 64; o <<= 1) q += __shfl_xor(q, o);
        const float mb = rbf(mean), vb = rbf(q * (1.f / D_));
        const float sd = rbf(sqrtf(rbf(vb + 1e-5f)));
#pragma unroll
        for (int c = 0; c < 2; ++c) {
            const int ch = lane + 64 * c;
            const int ks = ch >> 2, cp = ch & 3;
            float o[8];
#pragma unroll
            for (int e = 0; e < 8; ++e) o[e] = rbf(v[c * 8 + e] - mb) / sd;
            u16* ub = xn + ((size_t)(((m >> 8) * 32 + ks) * 16 + ((m >> 4) & 15))) * 512;
            const int g0 = (cp >> 1) * 2, j0 = (cp & 1) * 4;
            u32x2 w0 = {pk_bf16(o[0], o[1]), pk_bf16(o[2], o[3])};
            u32x2 w1 = {pk_bf16(o[4], o[5]), pk_bf16(o[6], o[7])};
            *(u32x2*)(ub + ((m & 15) + 16 * g0) * 8 + j0) = w0;
            *(u32x2*)(ub + ((m & 15) + 16 * (g0 + 1)) * 8 + j0) = w1;
        }
    }
}

constexpr int G1_NS = 4, G1_STAGE = 16384 + 13312, G1_LDS = G1_NS * G1_STAGE;

template <int TN>
DI void gemm1_body(const u16* __restrict__ xn, const u16* __restrict__ wqkv, const u16* __restrict__ w1, char* ws,
                   char* smem, int mb, int nb, int wid, int lane) {
    const int wr = wid & 3, wc = wid >> 2;
    const unsigned lds0 = (unsigned)(uintptr_t)(lds_cptr)smem;
    const char* aSrc = (const char*)xn + (size_t)mb * (32 * 16384) + wid * 2048 + lane * 16;
    const int lr = lane >= 26 ? 1 : 0, cc = (lane < 52) ? lane - 26 * lr : 0;
    const int col = nb * 208 + cc * 8;
    const unsigned bstride = col < NQKV ? 2u * NQKV : 2u * DFF;
    const char* bSrc = (col < NQKV ? (const char*)wqkv + col * 2 : (const char*)w1 + (col - NQKV) * 2) + (size_t)(4 * wid + lr) * bstride;
    const int g = lane >> 4, q = (lane & 15) >> 2, p = lane & 3;
    const lds_cptr aBase = (lds_cptr)smem + wr * 4096 + lane * 16;
    const lds_cptr bBase = (lds_cptr)smem + 16384 + (16 * (g >> 1) + 4 * (g & 1) + q) * 416 + p * 8 + wc * (7 * 32);

#define G1_ISSUE(slot) do { const unsigned sb_ = lds0 + (slot) * G1_STAGE; \
        glds16x2(aSrc, sb_ + wid * 2048); \
        if (lane < 52) { glds16(bSrc, sb_ + 16384 + wid * 1664); glds16(bSrc + 2 * bstride, sb_ + 16384 + wid * 1664 + 832); } \
        aSrc += 16384; bSrc += 32 * bstride; } while (0)
#define G1_LOAD(FA, FB, slot) do { const lds_cptr a_ = aBase + (slot) * G1_STAGE; const lds_cptr b_ = bBase + (slot) * G1_STAGE; \
        _Pragma("unroll") for (int mt = 0; mt < 4; ++mt) FA[mt] = *(lds_b128)(a_ + mt * 1024); \
        _Pragma("unroll") for (int nt = 0; nt < TN; ++nt) FB[nt] = cat8(tr_read(b_ + nt * 32), tr_read(b_ + 3328 + nt * 32)); } while (0)
#define G1_MMA(FA, FB) do { \
        _Pragma("unroll") for (int nt = 0; nt < TN; ++nt) _Pragma("unroll") for (int mt = 0; mt < 4; ++mt) \
            acc[mt][nt] = MFMA16(FB[nt], FA[mt], acc[mt][nt]); } while (0)

    f32x4 acc[4][TN];
#pragma unroll
    for (int mt = 0; mt < 4; ++mt)
#pragma unroll
        for (int nt = 0; nt < TN; ++nt) acc[mt][nt] = (f32x4){0.f, 0.f, 0.f, 0.f};
    bf16x8 fa0[4], fb0[TN], fa1[4], fb1[TN];

#pragma unroll
    for (int s = 0; s < G1_NS; ++s) G1_ISSUE(s);
    WAITBAR(12);
    G1_LOAD(fa0, fb0, 0);
#define G1_STEP(s, FAc, FBc, FAn, FBn) do { \
        if ((s) <= 28) WAITBAR(8); else if ((s) == 29) WAITBAR(4); else if ((s) == 30) WAITBAR(0); \
        if ((s) + G1_NS < 32) G1_ISSUE((s) & (G1_NS - 1)); \
        if ((s) < 31) G1_LOAD(FAn, FBn, ((s) + 1) & (G1_NS - 1)); \
        G1_MMA(FAc, FBc); } while (0)
    for (int s = 0; s < 32; s += 2) {
        G1_STEP(s, fa0, fb0, fa1, fb1);
        G1_STEP(s + 1, fa1, fb1, fa0, fb0);
    }
    const int fr = lane & 15, fq = lane >> 4;
    const float qscale = 0.17677669529663687f * 1.4426950408889634f;
#pragma unroll
    for (int nt = 0; nt < TN; ++nt) {
        const int N0 = nb * 208 + (wc * 7 + nt) * 16;
#pragma unroll
        for (int mt = 0; mt < 4; ++mt) {
            const int m = mb * 256 + wr * 64 + mt * 16 + fr;
            f32x4 v = acc[mt][nt];
            u16* dst;
            const int fo = ((m & 31) + 32 * (fq >> 1)) * 8 + (fq & 1) * 4;
            if (N0 < 2048) {
                const int hd = (N0 & 1023) >> 5, db = (N0 >> 4) & 1;
                if (N0 < 1024) v = v * qscale;
                dst = (u16*)(ws + (N0 < 1024 ? WS_Q : WS_K)) + (size_t)((hd * 64 + (m >> 5)) * 2 + db) * 512 + fo;
            } else if (N0 < NQKV) {
                const int hd = (N0 - 2048) >> 5, db = (N0 >> 4) & 1;
                dst = (u16*)(ws + WS_V) + (size_t)(hd * S_ + m) * 32 + db * 16 + 4 * fq;
            } else {
#pragma unroll
                for (int i = 0; i < 4; ++i) v[i] = fmaxf(v[i], 0.f);
                dst = (u16*)(ws + WS_A2) + (size_t)((m >> 5) * KB2 + 64 + ((N0 - NQKV) >> 4)) * 512 + fo;
            }
            *(u32x2*)dst = (u32x2){pk_bf16(v[0], v[1]), pk_bf16(v[2], v[3])};
        }
    }
#undef G1_ISSUE
#undef G1_LOAD
#undef G1_MMA
#undef G1_STEP
}

__global__ __launch_bounds__(512, 2) void gemm1_kernel(const u16* __restrict__ xn, const u16* __restrict__ wqkv,
                                                       const u16* __restrict__ w1, char* ws) {
    extern __shared__ __attribute__((aligned(16))) char smem[];
    const int wid = __builtin_amdgcn_readfirstlane(threadIdx.x >> 6), lane = threadIdx.x & 63;
    const int x = blockIdx.x & 7, idx = blockIdx.x >> 3;
    const int mb = (x & 1) * 4 + (idx & 3), nb = (x >> 1) * 8 + (idx >> 2);
    if (wid < 4) gemm1_body<7>(xn, wqkv, w1, ws, smem, mb, nb, wid, lane);
    else gemm1_body<6>(xn, wqkv, w1, ws, smem, mb, nb, wid, lane);
}

constexpr int AT_NS = 4, AT_STAGE = 16384, AT_LDS = AT_NS * AT_STAGE;
constexpr float AT_THR = 1073741824.f;

DI void pack_p(const f32x16& s, bf16x8& p0, bf16x8& p1) {
    u32x4 a = {pk_bf16(s[0], s[1]), pk_bf16(s[2], s[3]), pk_bf16(s[4], s[5]), pk_bf16(s[6], s[7])};
    u32x4 b = {pk_bf16(s[8], s[9]), pk_bf16(s[10], s[11]), pk_bf16(s[12], s[13]), pk_bf16(s[14], s[15])};
    p0 = __builtin_bit_cast(bf16x8, a); p1 = __builtin_bit_cast(bf16x8, b);
}
DI float rowmax16(const f32x16& s) {
    float a = fmaxf(fmaxf(s[0], s[1]), s[2]), b = fmaxf(fmaxf(s[3], s[4]), s[5]);
    a = fmaxf(fmaxf(a, s[6]), s[7]); b = fmaxf(fmaxf(b, s[8]), s[9]);
    a = fmaxf(fmaxf(a, s[10]), s[11]); b = fmaxf(fmaxf(b, s[12]), s[13]);
    a = fmaxf(fmaxf(a, s[14]), s[15]);
    a = fmaxf(a, b);
    return fmaxf(a, __shfl_xor(a, 32));
}

__global__ __launch_bounds__(512, 2) void attn_kernel(char* ws) {
    extern __shared__ __attribute__((aligned(16))) char smem[];
    const int wid = __builtin_amdgcn_readfirstlane(threadIdx.x >> 6), lane = threadIdx.x & 63;
    const int x = blockIdx.x & 7, idx = blockIdx.x >> 3;
    const int head = x * 4 + (idx >> 3), qt = idx & 7;
    const unsigned lds0 = (unsigned)(uintptr_t)(lds_cptr)smem;
    const char* kG = ws + WS_K + (size_t)head * 131072;
    const char* kSrc = kG + wid * 1024 + lane * 16;
    const char* vSrc = ws + WS_V + (size_t)head * 131072 + wid * 1024 + lane * 16;
#define AT_ISSUE(slot) do { const unsigned sb_ = lds0 + (slot) * AT_STAGE + wid * 1024; \
        glds16(kSrc, sb_); glds16(vSrc, sb_ + 8192); kSrc += 8192; vSrc += 8192; } while (0)
    AT_ISSUE(0); AT_ISSUE(1);
    const int rb = qt * 8 + wid;
    const bf16x8* qp = (const bf16x8*)(ws + WS_Q + (size_t)((head * 64 + rb) * 2) * 1024) + lane;
    const bf16x8 qf0 = qp[0], qf1 = qp[64];
    const int g = lane >> 4, q = (lane & 15) >> 2, p = lane & 3;
    const lds_cptr kBase = (lds_cptr)smem + lane * 16;
    const lds_cptr vBase = (lds_cptr)smem + 8192 + (4 * (g >> 1) + q) * 64 + (g & 1) * 32 + p * 8;
    const short one = ((g & 1) == ((lane >> 2) & 1)) ? (short)0x3f80 : (short)0;
    const bf16x8 ones = {one, one, one, one, one, one, one, one};
    f32x16 o, cm, sA, sB, zero16;
#pragma unroll
    for (int i = 0; i < 16; ++i) { o[i] = 0.f; zero16[i] = 0.f; }
    f32x4 l = {0.f, 0.f, 0.f, 0.f};
    float mref;
    WAITBAR(2);
    AT_ISSUE(2);
    {
        const bf16x8 k0 = *(lds_b128)(kBase), k1 = *(lds_b128)(kBase + 1024);
        sA = MFMA32(k0, qf0, zero16); sA = MFMA32(k1, qf1, sA);
        mref = rowmax16(sA);
#pragma unroll
        for (int i = 0; i < 16; ++i) { cm[i] = -mref; sA[i] -= mref; }
    }
#define AT_STEP(SC, SN, t, koff, voff, LAST) do { \
        if (!(LAST)) { const bf16x8 k0 = *(lds_b128)(kBase + (koff)), k1 = *(lds_b128)(kBase + (koff) + 1024); \
                       SN = MFMA32(k0, qf0, cm); SN = MFMA32(k1, qf1, SN); } \
        const bf16x8 v0 = cat8(tr_read(vBase + (voff)), tr_read(vBase + (voff) + 512)); \
        const bf16x8 v1 = cat8(tr_read(vBase + (voff) + 1024), tr_read(vBase + (voff) + 1536)); \
        _Pragma("unroll") for (int i = 0; i < 16; ++i) SC[i] = __builtin_amdgcn_exp2f(SC[i]); \
        bf16x8 p0, p1; pack_p(SC, p0, p1); \
        f32x4 ln = MFMA16(ones, p0, l); ln = MFMA16(ones, p1, ln); \
        if (__builtin_expect(__any(!(ln[0] <= AT_THR)), 0)) { \
            const bf16x8* kg = (const bf16x8*)(kG + (size_t)(t) * 2048) + lane; \
            f32x16 sr = MFMA32(kg[0], qf0, zero16); sr = MFMA32(kg[64], qf1, sr); \
            const float mn = fmaxf(rowmax16(sr), mref + __builtin_amdgcn_logf(l[0])); \
            const float f = __builtin_amdgcn_exp2f(mref - mn), dl = mn - mref; \
            mref = mn; \
            _Pragma("unroll") for (int i = 0; i < 16; ++i) { o[i] *= f; cm[i] = -mn; sr[i] = __builtin_amdgcn_exp2f(sr[i] - mn); if (!(LAST)) SN[i] -= dl; } \
            l *= f; pack_p(sr, p0, p1); \
            ln = MFMA16(ones, p0, l); ln = MFMA16(ones, p1, ln); \
        } \
        l = ln; \
        o = MFMA32(v0, p0, o); o = MFMA32(v1, p1, o); } while (0)

    for (int st = 0; st < 16; ++st) {
        const int so = (st & 3) * AT_STAGE, sn = ((st + 1) & 3) * AT_STAGE;
        AT_STEP(sA, sB, st * 4 + 0, so + 2048, so, false);
        AT_STEP(sB, sA, st * 4 + 1, so + 4096, so + 2048, false);
        AT_STEP(sA, sB, st * 4 + 2, so + 6144, so + 4096, false);
        if (st < 15) {
            if (st < 14) WAITBAR(2); else WAITBAR(0);
            if (st + 3 < 16) AT_ISSUE((st + 3) & 3);
            AT_STEP(sB, sA, st * 4 + 3, sn, so + 6144, false);
        } else {
            AT_STEP(sB, sA, st * 4 + 3, sn, so + 6144, true);
        }
    }
    const float rl = 1.f / l[0];
    u16* ob = (u16*)(ws + WS_A2) + (size_t)(rb * KB2 + 2 * head) * 512 + (lane & 31) * 8 + (lane >> 5) * 4;
#pragma unroll
    for (int gg = 0; gg < 4; ++gg)
        *(u32x2*)(ob + (gg >> 1) * 512 + (gg & 1) * 256) =
            (u32x2){pk_bf16(o[4 * gg] * rl, o[4 * gg + 1] * rl), pk_bf16(o[4 * gg + 2] * rl, o[4 * gg + 3] * rl)};
#undef AT_ISSUE
#undef AT_STEP
}

constexpr int G2_NS = 4, G2_STAGE = 8192 + 8704, G2_LDS = G2_NS * G2_STAGE;

__global__ __launch_bounds__(256, 2) void gemm2_kernel(const u16* __restrict__ wo, const u16* __restrict__ w2, char* ws) {
    extern __shared__ __attribute__((aligned(16))) char smem[];
    const int wid = __builtin_amdgcn_readfirstlane(threadIdx.x >> 6), lane = threadIdx.x & 63;
    const int x = blockIdx.x & 7, idx = blockIdx.x >> 3;
    const int split = idx & 1, mb = (x & 3) * 4 + ((idx >> 1) & 3), nb = (x >> 2) * 4 + (idx >> 3);
    constexpr int NST = 72;
    const int k0 = split * 2304;
    const int wr = wid & 1, wc = wid >> 1;
    const unsigned lds0 = (unsigned)(uintptr_t)(lds_cptr)smem;
    const char* aSrc = ws + WS_A2 + (size_t)((mb * 4 + wid) * KB2 + (k0 >> 4)) * 1024 + lane * 16;
    const unsigned bOff = (unsigned)(2 * wid + 8 * (lane >> 4)) * 2048u + nb * 256 + (lane & 15) * 16;
    const int g = lane >> 4, q = (lane & 15) >> 2, p = lane & 3;
    const lds_cptr aBase = (lds_cptr)smem + wr * 4096 + lane * 16;
    const lds_cptr bBase = (lds_cptr)smem + 8192 + q * 1088 + (g >> 1) * 256 + wc * 128 + (g & 1) * 32 + p * 8;
    int kn = k0;
#define G2_ISSUE(slot) do { const unsigned sb_ = lds0 + (slot) * G2_STAGE; \
        glds16x2(aSrc, sb_ + wid * 2048); \
        const char* wb_ = (kn < 1024 ? (const char*)wo + (size_t)kn * 2048 : (const char*)w2 + (size_t)(kn - 1024) * 2048) + bOff; \
        glds16(wb_, sb_ + 8192 + (2 * wid) * 1088); glds16(wb_ + 2048, sb_ + 8192 + (2 * wid + 1) * 1088); \
        aSrc += 2048; kn += 32; } while (0)
#define G2_LOAD(FA, FB, slot) do { const lds_cptr a_ = aBase + (slot) * G2_STAGE; const lds_cptr b_ = bBase + (slot) * G2_STAGE; \
        _Pragma("unroll") for (int ks = 0; ks < 2; ++ks) { \
            _Pragma("unroll") for (int mt = 0; mt < 2; ++mt) FA[ks][mt] = *(lds_b128)(a_ + mt * 2048 + ks * 1024); \
            _Pragma("unroll") for (int nt = 0; nt < 2; ++nt) FB[ks][nt] = cat8(tr_read(b_ + ks * 512 + nt * 64), tr_read(b_ + 4352 + ks * 512 + nt * 64)); } } while (0)
#define G2_MMA(FA, FB) do { \
        _Pragma("unroll") for (int ks = 0; ks < 2; ++ks) _Pragma("unroll") for (int nt = 0; nt < 2; ++nt) _Pragma("unroll") for (int mt = 0; mt < 2; ++mt) \
            acc[mt][nt] = MFMA32(FB[ks][nt], FA[ks][mt], acc[mt][nt]); } while (0)
    f32x16 acc[2][2];
#pragma unroll
    for (int mt = 0; mt < 2; ++mt)
#pragma unroll
        for (int nt = 0; nt < 2; ++nt)
#pragma unroll
            for (int i = 0; i < 16; ++i) acc[mt][nt][i] = 0.f;
    bf16x8 fa0[2][2], fb0[2][2], fa1[2][2], fb1[2][2];
#pragma unroll
    for (int s = 0; s < G2_NS; ++s) G2_ISSUE(s);
    WAITBAR(12);
    G2_LOAD(fa0, fb0, 0);
#define G2_STEP(s, FAc, FBc, FAn, FBn) do { \
        if ((s) <= NST - 4) WAITBAR(8); else if ((s) == NST - 3) WAITBAR(4); else if ((s) == NST - 2) WAITBAR(0); \
        if ((s) + G2_NS < NST) G2_ISSUE((s) & (G2_NS - 1)); \
        if ((s) < NST - 1) G2_LOAD(FAn, FBn, ((s) + 1) & (G2_NS - 1)); \
        G2_MMA(FAc, FBc); } while (0)
    for (int s = 0; s < NST; s += 2) {
        G2_STEP(s, fa0, fb0, fa1, fb1);
        G2_STEP(s + 1, fa1, fb1, fa0, fb0);
    }
    float* pb = (float*)(ws + WS_P) + (size_t)split * (S_ * D_);
#pragma unroll
    for (int mt = 0; mt < 2; ++mt)
#pragma unroll
        for (int nt = 0; nt < 2; ++nt) {
            float* d = pb + (size_t)((mb * 4 + wr * 2 + mt) * 32 + nb * 4 + wc * 2 + nt) * 1024 + lane * 4;
#pragma unroll
            for (int gg = 0; gg < 4; ++gg)
                *(f32x4*)(d + gg * 256) = (f32x4){acc[mt][nt][4 * gg], acc[mt][nt][4 * gg + 1], acc[mt][nt][4 * gg + 2], acc[mt][nt][4 * gg + 3]};
        }
#undef G2_ISSUE
#undef G2_LOAD
#undef G2_MMA
#undef G2_STEP
}

__global__ __launch_bounds__(256) void reduce_kernel(const u16* __restrict__ x, const char* ws, u16* __restrict__ out) {
    const int t = blockIdx.x * 256 + threadIdx.x;
    const int lane = t & 63, gg = (t >> 6) & 3, nb = (t >> 8) & 31, mb = t >> 13;
    const f32x4 a = ((const f32x4*)(ws + WS_P))[t], b = ((const f32x4*)(ws + WS_P + (size_t)S_ * D_ * 4))[t];
    const size_t off = (size_t)(mb * 32 + (lane & 31)) * D_ + nb * 32 + 8 * gg + 4 * (lane >> 5);
    const u32x2 xv = *(const u32x2*)(x + off);
    const float x0 = __uint_as_float(xv[0] << 16), x1 = __uint_as_float(xv[0] & 0xffff0000u);
    const float x2 = __uint_as_float(xv[1] << 16), x3 = __uint_as_float(xv[1] & 0xffff0000u);
    *(u32x2*)(out + off) = (u32x2){pk_bf16(x0 + a[0] + b[0], x1 + a[1] + b[1]), pk_bf16(x2 + a[2] + b[2], x3 + a[3] + b[3])};
}

extern "C" void kernel_launch(void* const* d_in, const int* in_sizes, int n_in, void* d_out, int out_size, void* d_ws,
                              size_t ws_size, hipStream_t stream) {
    const u16* x = (const u16*)d_in[0];
    const u16* wqkv = (const u16*)d_in[1];
    const u16* wo = (const u16*)d_in[2];
    const u16* w1 = (const u16*)d_in[4];
    const u16* w2 = (const u16*)d_in[5];
    char* ws = (char*)d_ws;
    (void)hipFuncSetAttribute((const void*)gemm1_kernel, hipFuncAttributeMaxDynamicSharedMemorySize, G1_LDS);
    (void)hipFuncSetAttribute((const void*)attn_kernel, hipFuncAttributeMaxDynamicSharedMemorySize, AT_LDS);
    (void)hipFuncSetAttribute((const void*)gemm2_kernel, hipFuncAttributeMaxDynamicSharedMemorySize, G2_LDS);
    ln_kernel<<<256, 256, 0, stream>>>(x, (u16*)(ws + WS_XN));
    gemm1_kernel<<<256, 512, G1_LDS, stream>>>((const u16*)(ws + WS_XN), wqkv, w1, ws);
    attn_kernel<<<256, 512, AT_LDS, stream>>>(ws);
    gemm2_kernel<<<256, 256, G2_LDS, stream>>>(wo, w2, ws);
    reduce_kernel<<<(S_ * D_ / 4) / 256, 256, 0, stream>>>(x, ws, (u16*)d_out);
}
```

```cpp
#include <hip/hip_runtime.h>
#include <hip/hip_bf16.h>
#include <stdint.h>

typedef unsigned short u16;
using bf16x8 = __attribute__((ext_vector_type(8))) short;
using s16x4  = __attribute__((ext_vector_type(4))) short;
using f32x4  = __attribute__((ext_vector_type(4))) float;
using f32x2  = __attribute__((ext_vector_type(2))) float;
using f32x16 = __attribute__((ext_vector_type(16))) float;
using u32x4  = __attribute__((ext_vector_type(4))) unsigned;
using u32x2  = __attribute__((ext_vector_type(2))) unsigned;
typedef __bf16 bf16x2_t __attribute__((ext_vector_type(2)));
typedef __attribute__((address_space(3))) const char* lds_cptr;
typedef __attribute__((address_space(3))) const bf16x8* lds_b128;
typedef __attribute__((address_space(3))) s16x4* lds_tr;
#define DI __device__ __forceinline__

constexpr int S_ = 2048, D_ = 1024, H_ = 32, DFF = 3584, NQKV = 3072;
constexpr size_t WS_XN = 0, WS_Q = 4u << 20, WS_K = 8u << 20, WS_V = 12u << 20, WS_A2 = 16u << 20, WS_H = 20u << 20, WS_P = 40u << 20;
constexpr int KB2 = 64;

#define ROT 0
#define STM 0
#define ABL2 0
#define ABL1 0
DI unsigned pk_bf16(float lo, float hi) {
    bf16x2_t r = __builtin_convertvector((f32x2){lo, hi}, bf16x2_t);
    return __builtin_bit_cast(unsigned, r);
}
DI float bf2f(u16 v) { return __uint_as_float((unsigned)v << 16); }
DI float rbf(float v) { return __uint_as_float(pk_bf16(v, 0.f) << 16); }

DI void glds16(const void* g, unsigned lds_base) {
    unsigned sv;
    asm volatile("s_mov_b32 %0, m0\n\ts_mov_b32 m0, %2\n\ts_nop 0\n\tglobal_load_lds_dwordx4 %1, off\n\ts_mov_b32 m0, %0"
                 : "=&s"(sv) : "v"(g), "s"(lds_base));
}
DI void glds16x2(const void* g, unsigned lds_base) {
    unsigned sv;
    asm volatile("s_mov_b32 %0, m0\n\ts_mov_b32 m0, %2\n\ts_nop 0\n\tglobal_load_lds_dwordx4 %1, off\n\t"
                 "global_load_lds_dwordx4 %1, off offset:1024\n\ts_mov_b32 m0, %0"
                 : "=&s"(sv) : "v"(g), "s"(lds_base));
}
DI void st8(void* p, u32x2 v) {
    if (STM == 1) __builtin_nontemporal_store(v, (u32x2*)p);
    else if (STM == 2) asm volatile("global_store_dwordx2 %0, %1, off sc1" :: "v"(p), "v"(v) : "memory");
    else if (STM == 3) asm volatile("global_store_dwordx2 %0, %1, off sc0 sc1" :: "v"(p), "v"(v) : "memory");
    else *(u32x2*)p = v;
}
DI s16x4 tr_read(lds_cptr p) { return __builtin_amdgcn_ds_read_tr16_b64_v4i16((lds_tr)p); }
DI bf16x8 cat8(s16x4 lo, s16x4 hi) { return __builtin_shufflevector(lo, hi, 0, 1, 2, 3, 4, 5, 6, 7); }
#define STAMP(ws, kid, i) do {} while (0)
#define MFMA16(a, b, c) __builtin_amdgcn_mfma_f32_16x16x32_bf16((a), (b), (c), 0, 0, 0)
#define MFMA32(a, b, c) __builtin_amdgcn_mfma_f32_32x32x16_bf16((a), (b), (c), 0, 0, 0)
#define WAITBAR(N) do { __builtin_amdgcn_s_waitcnt(0xC07F); asm volatile("s_waitcnt vmcnt(" #N ")\n\ts_barrier" ::: "memory"); } while (0)
#define SBAR() __builtin_amdgcn_sched_barrier(0)
#define SGB(mask, n) __builtin_amdgcn_sched_group_barrier((mask), (n), 0)

__global__ __launch_bounds__(512) void ln_kernel(const u16* __restrict__ x, u16* __restrict__ xn) {
    const int lane = threadIdx.x & 63, wid = threadIdx.x >> 6;
    const int m = blockIdx.x * 8 + wid;
    const u32x4* src = (const u32x4*)(x + (size_t)m * D_);
    const u32x4 raw[2] = {src[lane], src[lane + 64]};
    float v[16];
#pragma unroll
    for (int c = 0; c < 2; ++c)
#pragma unroll
        for (int i = 0; i < 4; ++i) {
            v[c * 8 + 2 * i] = __uint_as_float(raw[c][i] << 16);
            v[c * 8 + 2 * i + 1] = __uint_as_float(raw[c][i] & 0xffff0000u);
        }
    float s = 0.f;
#pragma unroll
    for (int i = 0; i < 16; ++i) s += v[i];
#pragma unroll
    for (int o = 1; o < 64; o <<= 1) s += __shfl_xor(s, o);
    const float mean = s * (1.f / D_);
    float q = 0.f;
#pragma unroll
    for (int i = 0; i < 16; ++i) { const float d = v[i] - mean; q += d * d; }
#pragma unroll
    for (int o = 1; o < 64; o <<= 1) q += __shfl_xor(q, o);
    const float mb = rbf(mean), vb = rbf(q * (1.f / D_));
    const float sd = rbf(sqrtf(rbf(vb + 1e-5f)));
#pragma unroll
    for (int c = 0; c < 2; ++c) {
        const int ch = lane + 64 * c;
        const int ks = ch >> 2, cp = ch & 3;
        float o[8];
#pragma unroll
        for (int e = 0; e < 8; ++e) o[e] = rbf(v[c * 8 + e] - mb) / sd;
        u16* ub = xn + ((size_t)(((m >> 8) * 32 + ks) * 16 + ((m >> 4) & 15))) * 512;
        const int g0 = (cp >> 1) * 2, j0 = (cp & 1) * 4;
        *(u32x2*)(ub + ((m & 15) + 16 * g0) * 8 + j0) = (u32x2){pk_bf16(o[0], o[1]), pk_bf16(o[2], o[3])};
        *(u32x2*)(ub + ((m & 15) + 16 * (g0 + 1)) * 8 + j0) = (u32x2){pk_bf16(o[4], o[5]), pk_bf16(o[6], o[7])};
    }
}

#define G1NS 5
constexpr int G1_NS = G1NS, G1_STAGE = 16384 + 13312, G1_LDS = G1_NS * G1_STAGE;

template <int TN>
DI void gemm1_body(const u16* __restrict__ xn, const u16* __restrict__ wqkv, const u16* __restrict__ w1, char* ws,
                   char* smem, int mb, int nb, int wid, int lane) {
    const int wr = wid & 3, wc = wid >> 2;
    const unsigned lds0 = (unsigned)(uintptr_t)(lds_cptr)smem;
    const char* aSrc = (const char*)xn + (size_t)mb * (32 * 16384) + wid * 2048 + lane * 16;
    const int lr = lane >= 26 ? 1 : 0, cc = (lane < 52) ? lane - 26 * lr : 0;
    const int col = nb * 208 + cc * 8;
    const unsigned bstride = col < NQKV ? 2u * NQKV : 2u * DFF;
    const char* bSrc = (col < NQKV ? (const char*)wqkv + col * 2 : (const char*)w1 + (col - NQKV) * 2) + (size_t)(4 * wid + lr) * bstride;
    const int g = lane >> 4, q = (lane & 15) >> 2, p = lane & 3;
    const lds_cptr aBase = (lds_cptr)smem + wr * 4096 + lane * 16;
    const lds_cptr bBase = (lds_cptr)smem + 16384 + (16 * (g >> 1) + 4 * (g & 1) + q) * 416 + p * 8 + wc * (7 * 32);

#define G1_PIECE(i, slot) do { const unsigned sb_ = lds0 + (slot) * G1_STAGE; \
        if ((i) == 0) glds16(aSrc, sb_ + wid * 2048); \
        if ((i) == 1) { glds16(aSrc + 1024, sb_ + wid * 2048 + 1024); aSrc += 16384; } \
        if ((i) == 2) { if (lane < 52) glds16(bSrc, sb_ + 16384 + wid * 1664); } \
        if ((i) == 3) { if (lane < 52) glds16(bSrc + 2 * bstride, sb_ + 16384 + wid * 1664 + 832); bSrc += 32 * bstride; } } while (0)
#define G1_ISSUE(slot) do { G1_PIECE(0, slot); G1_PIECE(1, slot); G1_PIECE(2, slot); G1_PIECE(3, slot); } while (0)
#define G1_RD(i, FA, FL, FH, a_, b_) do { if ((i) < 4) FA[(i) & 3] = *(lds_b128)(a_ + ((i) & 3) * 1024); \
        else if ((i) < 4 + 2 * TN) { if (((i) & 1) == 0) FL[(((i) - 4) >> 1) % TN] = tr_read(b_ + (((i) - 4) >> 1) * 32); else FH[(((i) - 4) >> 1) % TN] = tr_read(b_ + 3328 + (((i) - 4) >> 1) * 32); } } while (0)

    f32x4 acc[4][TN];
#pragma unroll
    for (int mt = 0; mt < 4; ++mt)
#pragma unroll
        for (int nt = 0; nt < TN; ++nt) acc[mt][nt] = (f32x4){0.f, 0.f, 0.f, 0.f};
    bf16x8 fa0[4], fa1[4];
    s16x4 fl0[TN], fh0[TN], fl1[TN], fh1[TN];

    STAMP(ws, 1, 0);
#pragma unroll
    for (int s = 0; s < G1_NS; ++s) G1_ISSUE(s);
    if (G1_NS == 5) WAITBAR(16); else WAITBAR(12);
    STAMP(ws, 1, 1);
#pragma unroll
    for (int i = 0; i < 4 + 2 * TN; ++i) G1_RD(i, fa0, fl0, fh0, aBase, bBase);
    int sl = 0;
#define G1_STEP(FAc, FLc, FHc, FAn, FLn, FHn, W, ISS, LD) do { \
        if ((W) == 3) WAITBAR(12); else if ((W) == 2) WAITBAR(8); else if ((W) == 1) WAITBAR(4); else if ((W) == 0) WAITBAR(0); \
        const int sn_ = sl == G1_NS - 1 ? 0 : sl + 1; \
        const lds_cptr a_ = aBase + sn_ * G1_STAGE; const lds_cptr b_ = bBase + sn_ * G1_STAGE; \
        _Pragma("unroll") for (int nt = 0; nt < TN; ++nt) { \
            const bf16x8 fb_ = cat8(FLc[nt], FHc[nt]); \
            _Pragma("unroll") for (int mt = 0; mt < 4; ++mt) { \
                acc[mt][nt] = MFMA16(fb_, FAc[mt], acc[mt][nt]); \
                if ((LD) && (mt < 3 || TN == 6)) { G1_RD(TN == 7 ? nt * 3 + mt : nt * 4 + mt, FAn, FLn, FHn, a_, b_); } \
                SBAR(); } \
            if (!ABL1 && (ISS) && nt >= 1 && nt <= 4) { G1_PIECE(nt - 1, sl); SBAR(); } \
        } \
        sl = sn_; } while (0)
    for (int s = 0; s < 26; s += 2) {
        G1_STEP(fa0, fl0, fh0, fa1, fl1, fh1, G1_NS - 2, true, true);
        G1_STEP(fa1, fl1, fh1, fa0, fl0, fh0, G1_NS - 2, true, true);
    }
    G1_STEP(fa0, fl0, fh0, fa1, fl1, fh1, G1_NS - 2, true, true);
    G1_STEP(fa1, fl1, fh1, fa0, fl0, fh0, G1_NS - 2, G1_NS == 4, true);
    G1_STEP(fa0, fl0, fh0, fa1, fl1, fh1, 2, false, true);
    G1_STEP(fa1, fl1, fh1, fa0, fl0, fh0, 1, false, true);
    G1_STEP(fa0, fl0, fh0, fa1, fl1, fh1, 0, false, true);
    G1_STEP(fa1, fl1, fh1, fa0, fl0, fh0, -1, false, false);
    STAMP(ws, 1, 2);
    int lane2 = threadIdx.x; asm volatile("" : "+v"(lane2)); lane2 &= 63;
    const int fr = lane2 & 15, fq = lane2 >> 4;
    const float qscale = 0.17677669529663687f * 1.4426950408889634f;
#pragma unroll
    for (int nt = 0; nt < TN; ++nt) {
        const int N0 = nb * 208 + (wc * 7 + nt) * 16;
#pragma unroll
        for (int mt = 0; mt < 4; ++mt) {
            const int m = mb * 256 + wr * 64 + mt * 16 + fr;
            f32x4 v = acc[mt][nt];
            u16* dst;
            const int fo = ((m & 31) + 32 * (fq >> 1)) * 8 + (fq & 1) * 4;
            if (N0 < 2048) {
                const int hd = (N0 & 1023) >> 5, db = (N0 >> 4) & 1;
                if (N0 < 1024) v = v * qscale;
                dst = (u16*)(ws + (N0 < 1024 ? WS_Q : WS_K)) + (size_t)((hd * 64 + (m >> 5)) * 2 + db) * 512 + fo;
            } else if (N0 < NQKV) {
                const int hd = (N0 - 2048) >> 5, db = (N0 >> 4) & 1;
                dst = (u16*)(ws + WS_V) + (size_t)(hd * S_ + m) * 32 + db * 16 + 4 * fq;
            } else {
#pragma unroll
                for (int i = 0; i < 4; ++i) v[i] = fmaxf(v[i], 0.f);
                const int kk = N0 - NQKV;
                dst = (u16*)(ws + WS_H) + (size_t)((m >> 4) * 112 + (kk >> 5)) * 512 + ((m & 15) + 16 * (2 * ((kk >> 4) & 1) + (fq & 1))) * 8 + 4 * (fq >> 1);
            }
            st8(dst, (u32x2){pk_bf16(v[0], v[1]), pk_bf16(v[2], v[3])});
        }
    }
    STAMP(ws, 1, 3);
#undef G1_PIECE
#undef G1_ISSUE
#undef G1_RD
#undef G1_STEP
}

__global__ __launch_bounds__(512, 2) void gemm1_kernel(const u16* __restrict__ xn, const u16* __restrict__ wqkv,
                                                       const u16* __restrict__ w1, char* ws) {
    extern __shared__ __attribute__((aligned(16))) char smem[];
    const int wid = __builtin_amdgcn_readfirstlane(threadIdx.x >> 6), lane = threadIdx.x & 63;
    const int xc = blockIdx.x & 7, idx = blockIdx.x >> 3;
    const int mb = (xc & 1) * 4 + (idx & 3), nb = (xc >> 1) * 8 + (idx >> 2);
    if (wid < 4) gemm1_body<7>(xn, wqkv, w1, ws, smem, mb, nb, wid, lane);
    else gemm1_body<6>(xn, wqkv, w1, ws, smem, mb, nb, wid, lane);
}

constexpr int AT_NS = 4, AT_STAGE = 16384, AT_LDS = AT_NS * AT_STAGE;
constexpr float AT_THR = 1.8446744e19f;

DI void pack_p(const f32x16& s, bf16x8& p0, bf16x8& p1) {
    u32x4 a = {pk_bf16(s[0], s[1]), pk_bf16(s[2], s[3]), pk_bf16(s[4], s[5]), pk_bf16(s[6], s[7])};
    u32x4 b = {pk_bf16(s[8], s[9]), pk_bf16(s[10], s[11]), pk_bf16(s[12], s[13]), pk_bf16(s[14], s[15])};
    p0 = __builtin_bit_cast(bf16x8, a); p1 = __builtin_bit_cast(bf16x8, b);
}
DI float rowmax16(const f32x16& s) {
    float a = fmaxf(fmaxf(s[0], s[1]), s[2]), b = fmaxf(fmaxf(s[3], s[4]), s[5]);
    a = fmaxf(fmaxf(a, s[6]), s[7]); b = fmaxf(fmaxf(b, s[8]), s[9]);
    a = fmaxf(fmaxf(a, s[10]), s[11]); b = fmaxf(fmaxf(b, s[12]), s[13]);
    a = fmaxf(fmaxf(a, s[14]), s[15]);
    a = fmaxf(a, b);
    return fmaxf(a, __shfl_xor(a, 32));
}

DI void attn_slow(char* ws, int head, int lane, bf16x8 q0, bf16x8 q1, bf16x8 ones, f32x16& o, f32x4& l) {
    const bf16x8* kg = (const bf16x8*)(ws + WS_K + (size_t)head * 131072) + lane;
    const u16* vg = (const u16*)(ws + WS_V + (size_t)head * 131072) + (lane & 31) + (lane >> 5) * 128;
    f32x16 zero16;
#pragma unroll
    for (int i = 0; i < 16; ++i) { o[i] = 0.f; zero16[i] = 0.f; }
    l = (f32x4){0.f, 0.f, 0.f, 0.f};
    float m = -3.0e38f;
#pragma unroll 1
    for (int t = 0; t < 64; ++t) {
        f32x16 sr = MFMA32(kg[t * 128], q0, zero16); sr = MFMA32(kg[t * 128 + 64], q1, sr);
        const float mn = fmaxf(rowmax16(sr), m), f = __builtin_amdgcn_exp2f(m - mn);
        m = mn;
#pragma unroll
        for (int i = 0; i < 16; ++i) { o[i] *= f; sr[i] = __builtin_amdgcn_exp2f(sr[i] - mn); }
        l *= f;
        bf16x8 p0, p1, v0, v1; pack_p(sr, p0, p1);
#pragma unroll
        for (int j = 0; j < 8; ++j) {
            v0[j] = (short)vg[(t * 32 + 8 * (j >> 2) + (j & 3)) * 32];
            v1[j] = (short)vg[(t * 32 + 16 + 8 * (j >> 2) + (j & 3)) * 32];
        }
        l = MFMA16(ones, p0, l); l = MFMA16(ones, p1, l);
        o = MFMA32(v0, p0, o); o = MFMA32(v1, p1, o);
    }
}

DI void attn_body(char* ws, char* smem, int head, int qt, int wid, int lane) {
    const unsigned lds0 = (unsigned)(uintptr_t)(lds_cptr)smem;
    const char* kG = ws + WS_K + (size_t)head * 131072;
    const char* kSrc = kG + wid * 2048 + lane * 16;
    const char* vSrc = ws + WS_V + (size_t)head * 131072 + wid * 2048 + lane * 16;
#define AT_PIECE(i, slot) do { const unsigned sb_ = lds0 + (slot) * AT_STAGE + wid * 2048; \
        if ((i) == 0) glds16(kSrc, sb_); \
        if ((i) == 1) { glds16(kSrc + 1024, sb_ + 1024); kSrc += 8192; } \
        if ((i) == 2) glds16(vSrc, sb_ + 8192); \
        if ((i) == 3) { glds16(vSrc + 1024, sb_ + 9216); vSrc += 8192; } } while (0)
#define AT_ISSUE(slot) do { AT_PIECE(0, slot); AT_PIECE(1, slot); AT_PIECE(2, slot); AT_PIECE(3, slot); } while (0)
    STAMP(ws, 2, 0);
    AT_ISSUE(0); AT_ISSUE(1);
    const int rb = qt * 8 + wid * 2;
    const bf16x8* qp = (const bf16x8*)(ws + WS_Q + (size_t)((head * 64 + rb) * 2) * 1024) + lane;
    const bf16x8 qA0 = qp[0], qA1 = qp[64], qB0 = qp[128], qB1 = qp[192];
    const int g = lane >> 4, q = (lane & 15) >> 2, p = lane & 3;
    const lds_cptr kBase = (lds_cptr)smem + lane * 16;
    const lds_cptr vBase = (lds_cptr)smem + 8192 + (4 * (g >> 1) + q) * 64 + (g & 1) * 32 + p * 8;
    const short one = ((g & 1) == ((lane >> 2) & 1)) ? (short)0x3f80 : (short)0;
    const bf16x8 ones = {one, one, one, one, one, one, one, one};
    f32x16 oA, oB, cmA, cmB, sA, sB, zero16;
#pragma unroll
    for (int i = 0; i < 16; ++i) { oA[i] = 0.f; oB[i] = 0.f; zero16[i] = 0.f; }
    f32x4 lA = {0.f, 0.f, 0.f, 0.f}, lB = lA;
    float mA, mB;
    bf16x8 k0, k1, v0, v1;
    u32x4 pA0, pA1, pB0, pB1;
    WAITBAR(4);
    STAMP(ws, 2, 1);
    AT_ISSUE(2);
    {
        k0 = *(lds_b128)(kBase); k1 = *(lds_b128)(kBase + 1024);
        sA = MFMA32(k0, qA0, zero16); sB = MFMA32(k0, qB0, zero16);
        sA = MFMA32(k1, qA1, sA); sB = MFMA32(k1, qB1, sB);
        mA = rowmax16(sA); mB = rowmax16(sB);
#pragma unroll
        for (int i = 0; i < 16; ++i) { cmA[i] = -mA; sA[i] -= mA; cmB[i] = -mB; sB[i] -= mB; }
    }
#define EX4(S, i) do { S[i] = __builtin_amdgcn_exp2f(S[i]); S[i + 1] = __builtin_amdgcn_exp2f(S[i + 1]); S[i + 2] = __builtin_amdgcn_exp2f(S[i + 2]); S[i + 3] = __builtin_amdgcn_exp2f(S[i + 3]); } while (0)
#define BC(x) __builtin_bit_cast(bf16x8, x)
#define EX(S, i) S[i] = __builtin_amdgcn_exp2f(S[i])
#define AT_PHASE(SX, PX0, PX1, SY, PY0, PY1, QY0, QY1, CMY, OY, LY, DOY, DOQK, RDV, voff, RDK, koff, GL, gslot) do { \
        EX(SX, 0); EX(SX, 1); EX(SX, 2); SBAR(); \
        if (DOQK) { if (ABL2 & 4) { asm volatile("" : "+v"(SY)); SY = MFMA32(k0, QY0, SY); } else SY = MFMA32(k0, QY0, CMY); } SBAR(); \
        EX(SX, 3); EX(SX, 4); EX(SX, 5); PX0[0] = pk_bf16(SX[0], SX[1]); SBAR(); \
        if (DOY) LY = MFMA16(ones, BC(PY0), LY); SBAR(); \
        EX(SX, 6); EX(SX, 7); EX(SX, 8); PX0[1] = pk_bf16(SX[2], SX[3]); SBAR(); \
        if (DOQK) SY = MFMA32(k1, QY1, SY); SBAR(); \
        if (!(ABL2 & 1) && (RDK)) { k0 = *(lds_b128)(kBase + (koff)); k1 = *(lds_b128)(kBase + (koff) + 1024); } \
        EX(SX, 9); EX(SX, 10); PX0[2] = pk_bf16(SX[4], SX[5]); PX0[3] = pk_bf16(SX[6], SX[7]); SBAR(); \
        if (DOY) OY = MFMA32(v0, BC(PY0), OY); SBAR(); \
        if (!(ABL2 & 2) && (GL) >= 0) { AT_PIECE(GL, gslot); SBAR(); } \
        EX(SX, 11); EX(SX, 12); EX(SX, 13); PX1[0] = pk_bf16(SX[8], SX[9]); SBAR(); \
        if (DOY) LY = MFMA16(ones, BC(PY1), LY); SBAR(); \
        EX(SX, 14); EX(SX, 15); PX1[1] = pk_bf16(SX[10], SX[11]); PX1[2] = pk_bf16(SX[12], SX[13]); SBAR(); \
        if (DOY) OY = MFMA32(v1, BC(PY1), OY); SBAR(); \
        if ((!(ABL2 & 1) || (voff) == 0) && (RDV)) { v0 = cat8(tr_read(vBase + (voff)), tr_read(vBase + (voff) + 512)); v1 = cat8(tr_read(vBase + (voff) + 1024), tr_read(vBase + (voff) + 1536)); } \
        PX1[3] = pk_bf16(SX[14], SX[15]); SBAR(); } while (0)
#define AT_TILE(t, FIRST, LAST, voff, koff, GL, gslot) do { \
        AT_PHASE(sA, pA0, pA1, sB, pB0, pB1, qB0, qB1, cmB, oB, lB, !(FIRST), !(FIRST), true, voff, !(LAST), koff, GL, gslot); \
        AT_PHASE(sB, pB0, pB1, sA, pA0, pA1, qA0, qA1, cmA, oA, lA, true, !(LAST), false, 0, false, 0, -1, 0); } while (0)

    AT_TILE(0, true, false, 0, 2048, -1, 0);
    AT_TILE(1, false, false, 2048, 4096, -1, 0);
    AT_TILE(2, false, false, 4096, 6144, -1, 0);
    WAITBAR(4);
    AT_TILE(3, false, false, 6144, AT_STAGE, 0, 3);
    for (int st = 1; st < 13; ++st) {
        const int so = (st & 3) * AT_STAGE, sn = ((st + 1) & 3) * AT_STAGE, gp = (st + 2) & 3, gn = (st + 3) & 3;
        AT_TILE(st * 4 + 0, false, false, so, so + 2048, 1, gp);
        AT_TILE(st * 4 + 1, false, false, so + 2048, so + 4096, 2, gp);
        AT_TILE(st * 4 + 2, false, false, so + 4096, so + 6144, 3, gp);
        WAITBAR(4);
        AT_TILE(st * 4 + 3, false, false, so + 6144, sn, 0, gn);
    }
    {
        const int so = AT_STAGE, sn = 2 * AT_STAGE;
        AT_TILE(52, false, false, so, so + 2048, 1, 3);
        AT_TILE(53, false, false, so + 2048, so + 4096, 2, 3);
        AT_TILE(54, false, false, so + 4096, so + 6144, 3, 3);
        WAITBAR(4);
        AT_TILE(55, false, false, so + 6144, sn, -1, 0);
    }
    {   const int so = 2 * AT_STAGE, sn = 3 * AT_STAGE;
        AT_TILE(56, false, false, so, so + 2048, -1, 0);
        AT_TILE(57, false, false, so + 2048, so + 4096, -1, 0);
        AT_TILE(58, false, false, so + 4096, so + 6144, -1, 0);
        WAITBAR(0);
        AT_TILE(59, false, false, so + 6144, sn, -1, 0);
    }
    {   const int so = 3 * AT_STAGE;
        AT_TILE(60, false, false, so, so + 2048, -1, 0);
        AT_TILE(61, false, false, so + 2048, so + 4096, -1, 0);
        AT_TILE(62, false, false, so + 4096, so + 6144, -1, 0);
        AT_TILE(63, false, true, so + 6144, 0, -1, 0);
    }
    lB = MFMA16(ones, BC(pB0), lB); lB = MFMA16(ones, BC(pB1), lB);
    oB = MFMA32(v0, BC(pB0), oB); oB = MFMA32(v1, BC(pB1), oB);
    STAMP(ws, 2, 2);
    {
        float chk = fmaxf(lA[0], lB[0]);
#pragma unroll
        for (int i = 0; i < 16; ++i) chk = fmaxf(chk, fmaxf(fabsf(oA[i]), fabsf(oB[i])) * 1e-19f);
        bool bad = !(chk <= AT_THR);
#pragma unroll
        for (int i = 0; i < 16; ++i) bad = bad || (oA[i] != oA[i]) || (oB[i] != oB[i]);
        if (__builtin_expect(__any(bad), 0)) {
            attn_slow(ws, head, lane, qA0, qA1, ones, oA, lA);
            attn_slow(ws, head, lane, qB0, qB1, ones, oB, lB);
        }
    }
    const float rA = 1.f / lA[0], rB = 1.f / lB[0];
    u16* ob = (u16*)(ws + WS_A2) + (size_t)(rb * KB2 + 2 * head) * 512 + (lane & 31) * 8 + (lane >> 5) * 4;
#pragma unroll
    for (int gg = 0; gg < 4; ++gg) {
        *(u32x2*)(ob + (gg >> 1) * 512 + (gg & 1) * 256) =
            (u32x2){pk_bf16(oA[4 * gg] * rA, oA[4 * gg + 1] * rA), pk_bf16(oA[4 * gg + 2] * rA, oA[4 * gg + 3] * rA)};
        *(u32x2*)(ob + KB2 * 512 + (gg >> 1) * 512 + (gg & 1) * 256) =
            (u32x2){pk_bf16(oB[4 * gg] * rB, oB[4 * gg + 1] * rB), pk_bf16(oB[4 * gg + 2] * rB, oB[4 * gg + 3] * rB)};
    }
    STAMP(ws, 2, 3);
#undef AT_PIECE
#undef AT_ISSUE
#undef EX4
#undef EX
#undef BC
#undef AT_PHASE
#undef AT_TILE
}

constexpr int G2_NS = 4, G2_STAGE = 8192 + 8448, G2_LDS = G2_NS * G2_STAGE;

DI void ffn2_body(const u16* __restrict__ x, const u16* __restrict__ w2, char* ws, char* smem, int split, int mb, int nb, int wid, int lane) {
    constexpr int NST = 56;
    const int wr = wid & 1, wc = wid >> 1;
    const unsigned lds0 = (unsigned)(uintptr_t)(lds_cptr)smem;
    const char* aSrc = ws + WS_H + (size_t)((mb * 8 + wid * 2) * 112 + split * NST) * 1024 + lane * 16;
    const char* bSrc = (const char*)w2 + (size_t)(split * 1792 + 2 * wid + 8 * (lane >> 4)) * 2048 + nb * 256 + (lane & 15) * 16;
    const int g = lane >> 4, q = (lane & 15) >> 2, p = lane & 3;
    const lds_cptr aBase = (lds_cptr)smem + wr * 4096 + lane * 16;
    const lds_cptr bBase = (lds_cptr)smem + 8192 + (4 * (g & 1) + q) * 1056 + (g >> 1) * 512 + wc * 128 + p * 8;
#define G2_PIECE(i, slot) do { const unsigned sb_ = lds0 + (slot) * G2_STAGE; \
        if ((i) == 0) glds16(aSrc, sb_ + wid * 2048); \
        if ((i) == 1) { glds16(aSrc + 112 * 1024, sb_ + wid * 2048 + 1024); aSrc += 1024; } \
        if ((i) == 2) glds16(bSrc, sb_ + 8192 + (2 * wid) * 1056); \
        if ((i) == 3) { glds16(bSrc + 2048, sb_ + 8192 + (2 * wid + 1) * 1056); bSrc += 65536; } } while (0)
#define G2_ISSUE(slot) do { G2_PIECE(0, slot); G2_PIECE(1, slot); G2_PIECE(2, slot); G2_PIECE(3, slot); } while (0)
#define G2_RD(i, FA, FL, FH, a_, b_) do { if ((i) < 4) FA[(i) & 3] = *(lds_b128)(a_ + ((i) & 3) * 1024); \
        else if (((i) & 1) == 0) FL[(((i) - 4) >> 1) & 3] = tr_read(b_ + (((i) - 4) >> 1) * 32); else FH[(((i) - 4) >> 1) & 3] = tr_read(b_ + 256 + (((i) - 4) >> 1) * 32); } while (0)
    f32x4 acc[4][4];
#pragma unroll
    for (int mt = 0; mt < 4; ++mt)
#pragma unroll
        for (int nt = 0; nt < 4; ++nt) acc[mt][nt] = (f32x4){0.f, 0.f, 0.f, 0.f};
    bf16x8 fa0[4], fa1[4];
    s16x4 fl0[4], fh0[4], fl1[4], fh1[4];
    STAMP(ws, 3, 0);
#pragma unroll
    for (int s = 0; s < G2_NS; ++s) G2_ISSUE(s);
    WAITBAR(12);
    STAMP(ws, 3, 1);
#pragma unroll
    for (int i = 0; i < 12; ++i) G2_RD(i, fa0, fl0, fh0, aBase, bBase);
#define G2_STEP(s, FAc, FLc, FHc, FAn, FLn, FHn, W, ISS, LD) do { \
        if ((W) == 8) WAITBAR(8); else if ((W) == 4) WAITBAR(4); else if ((W) == 0) WAITBAR(0); \
        const lds_cptr a_ = aBase + (((s) + 1) & (G2_NS - 1)) * G2_STAGE; const lds_cptr b_ = bBase + (((s) + 1) & (G2_NS - 1)) * G2_STAGE; \
        _Pragma("unroll") for (int nt = 0; nt < 4; ++nt) { \
            const bf16x8 fb_ = cat8(FLc[nt], FHc[nt]); \
            _Pragma("unroll") for (int mt = 0; mt < 4; ++mt) { \
                acc[mt][nt] = MFMA16(fb_, FAc[mt], acc[mt][nt]); \
                if ((LD) && nt < 3) { G2_RD(nt * 4 + mt, FAn, FLn, FHn, a_, b_); } \
                SBAR(); } \
            if (!ABL1 && (ISS)) { G2_PIECE(nt, (s) & (G2_NS - 1)); SBAR(); } \
        } } while (0)
    for (int s = 0; s < NST - 4; s += 2) {
        G2_STEP(s, fa0, fl0, fh0, fa1, fl1, fh1, 8, true, true);
        G2_STEP(s + 1, fa1, fl1, fh1, fa0, fl0, fh0, 8, true, true);
    }
    G2_STEP(NST - 4, fa0, fl0, fh0, fa1, fl1, fh1, 8, false, true);
    G2_STEP(NST - 3, fa1, fl1, fh1, fa0, fl0, fh0, 4, false, true);
    G2_STEP(NST - 2, fa0, fl0, fh0, fa1, fl1, fh1, 0, false, true);
    G2_STEP(NST - 1, fa1, fl1, fh1, fa0, fl0, fh0, -1, false, false);
    STAMP(ws, 3, 2);
    const int fr = lane & 15, fq = lane >> 4;
    u16* pb = (u16*)(ws + WS_P) + (size_t)split * (S_ * D_);
#pragma unroll
    for (int mt = 0; mt < 4; ++mt) {
        u32x2 xv[4];
        if (split == 0) {
#pragma unroll
            for (int nt = 0; nt < 4; ++nt) xv[nt] = *(const u32x2*)(x + (size_t)(mb * 128 + wr * 64 + mt * 16 + fr) * D_ + nb * 128 + wc * 64 + nt * 16 + 4 * fq);
        }
#pragma unroll
        for (int nt = 0; nt < 4; ++nt) {
            f32x4 v = acc[mt][nt];
            if (split == 0) {
                v[0] += __uint_as_float(xv[nt][0] << 16); v[1] += __uint_as_float(xv[nt][0] & 0xffff0000u);
                v[2] += __uint_as_float(xv[nt][1] << 16); v[3] += __uint_as_float(xv[nt][1] & 0xffff0000u);
            }
            u16* d = pb + (size_t)((mb * 4 + wr * 2 + (mt >> 1)) * 32 + nb * 4 + wc * 2 + (nt >> 1)) * 1024 + (nt & 1) * 512
                        + ((mt & 1) * 16 + fr + 32 * (fq & 1)) * 8 + (fq >> 1) * 4;
            *(u32x2*)d = (u32x2){pk_bf16(v[0], v[1]), pk_bf16(v[2], v[3])};
        }
    }
    STAMP(ws, 3, 3);
#undef G2_PIECE
#undef G2_ISSUE
#undef G2_RD
#undef G2_STEP
}

constexpr int K3_LDS = G2_LDS > AT_LDS ? G2_LDS : AT_LDS;
__global__ __launch_bounds__(256, 2) void k3_kernel(const u16* __restrict__ xin, const u16* __restrict__ w2, char* ws) {
    extern __shared__ __attribute__((aligned(16))) char smem[];
    const int wid = __builtin_amdgcn_readfirstlane(threadIdx.x >> 6), lane = threadIdx.x & 63;
    const int x = blockIdx.x & 7, idx = (blockIdx.x >> 3) & 31;
#define K3_ROLE 3
    if (blockIdx.x < 256) {
        if (K3_ROLE & 1) {
            __builtin_amdgcn_s_setprio(2);
            attn_body(ws, smem, x * 4 + (idx >> 3), idx & 7, wid, lane);
        }
    } else {
        if (K3_ROLE & 2) ffn2_body(xin, w2, ws, smem, x & 1, ((x >> 1) & 1) * 8 + (idx & 7), (x >> 2) * 4 + (idx >> 3), wid, lane);
    }
}

constexpr int G4_NS = 12, G4_STAGE = 8192 + 4352, G4_LDS = G4_NS * G4_STAGE;

__global__ __launch_bounds__(256, 1) void k4_kernel(const u16* __restrict__ x, const u16* __restrict__ wo, char* ws, u16* __restrict__ out) {
    extern __shared__ __attribute__((aligned(16))) char smem[];
    const int wid = __builtin_amdgcn_readfirstlane(threadIdx.x >> 6), lane = threadIdx.x & 63;
    const int xc = blockIdx.x & 7, idx = blockIdx.x >> 3;
    const int mb = (xc & 3) * 4 + (idx & 3), nb = (xc >> 2) * 8 + (idx >> 2);
    const int wr = wid & 1, wc = wid >> 1;
    const unsigned lds0 = (unsigned)(uintptr_t)(lds_cptr)smem;
    const char* aSrc = ws + WS_A2 + (size_t)((mb * 4 + wid) * KB2) * 1024 + lane * 16;
    const char* bSrc = (const char*)wo + (size_t)(wid + 4 * (lane >> 3)) * 2048 + nb * 128 + (lane & 7) * 16;
    const int g = lane >> 4, q = (lane & 15) >> 2, p = lane & 3;
    const lds_cptr aBase = (lds_cptr)smem + wr * 4096 + lane * 16;
    const lds_cptr bBase = (lds_cptr)smem + 8192 + q * 1088 + (g >> 1) * 256 + wc * 64 + (g & 1) * 32 + p * 8;
    STAMP(ws, 4, 0);
    int kpos = ROT ? (8 * (idx & 3) + 4 * (idx >> 2)) & 31 : 0;
    f32x16 acc[2];
#pragma unroll
    for (int i = 0; i < 16; ++i) { acc[0][i] = 0.f; acc[1][i] = 0.f; }
#define G4_ISSUE(slot) do { const unsigned sb_ = lds0 + (slot) * G4_STAGE; \
        glds16x2(aSrc + kpos * 2048, sb_ + wid * 2048); glds16(bSrc + kpos * 65536, sb_ + 8192 + wid * 1088); kpos = (kpos + 1) & 31; } while (0)
#define G4_LOAD(FA, FB, slot) do { const lds_cptr a_ = aBase + (slot) * G4_STAGE; const lds_cptr b_ = bBase + (slot) * G4_STAGE; \
        _Pragma("unroll") for (int ks = 0; ks < 2; ++ks) { \
            FA[ks][0] = *(lds_b128)(a_ + ks * 1024); FA[ks][1] = *(lds_b128)(a_ + 2048 + ks * 1024); \
            FB[ks] = cat8(tr_read(b_ + ks * 512), tr_read(b_ + ks * 512 + 128)); } } while (0)
    bf16x8 fa0[2][2], fb0[2], fa1[2][2], fb1[2];
#pragma unroll
    for (int s = 0; s < G4_NS; ++s) G4_ISSUE(s);
    u32x4 pr[2][2][2];
    {
        const u16* pb = (const u16*)(ws + WS_P);
#pragma unroll
        for (int mt = 0; mt < 2; ++mt) {
            const size_t t = (size_t)((mb * 4 + wr * 2 + mt) * 32 + nb * 2 + wc) * 1024 + lane * 8;
#pragma unroll
            for (int hh = 0; hh < 2; ++hh) {
                pr[mt][hh][0] = *(const u32x4*)(pb + t + hh * 512);
                pr[mt][hh][1] = *(const u32x4*)(pb + (size_t)S_ * D_ + t + hh * 512);
            }
        }
    }
    WAITBAR(41);
    STAMP(ws, 4, 1);
    G4_LOAD(fa0, fb0, 0);
    int slot = 0;
#define G4_STEP(s, FAc, FBc, FAn, FBn) do { \
        const int rem_ = 30 - (s);     \
        if ((s) <= 10) WAITBAR(38); else if (rem_ >= 10) WAITBAR(30); else if (rem_ >= 6) WAITBAR(18); else if (rem_ >= 3) WAITBAR(9); else if (rem_ >= 1) WAITBAR(3); else if (rem_ == 0) WAITBAR(0); \
        const int sn_ = slot == G4_NS - 1 ? 0 : slot + 1; \
        const lds_cptr a_ = aBase + sn_ * G4_STAGE; const lds_cptr b_ = bBase + sn_ * G4_STAGE; const unsigned sb_ = lds0 + slot * G4_STAGE; \
        acc[0] = MFMA32(FBc[0], FAc[0][0], acc[0]); \
        if ((s) < 31) { FAn[0][0] = *(lds_b128)(a_); FAn[0][1] = *(lds_b128)(a_ + 2048); FBn[0] = cat8(tr_read(b_), tr_read(b_ + 128)); } SBAR(); \
        acc[1] = MFMA32(FBc[0], FAc[0][1], acc[1]); \
        if ((s) < 31) { FAn[1][0] = *(lds_b128)(a_ + 1024); FAn[1][1] = *(lds_b128)(a_ + 3072); FBn[1] = cat8(tr_read(b_ + 512), tr_read(b_ + 640)); } SBAR(); \
        acc[0] = MFMA32(FBc[1], FAc[1][0], acc[0]); SBAR(); \
        if ((s) + G4_NS < 32) { glds16x2(aSrc + kpos * 2048, sb_ + wid * 2048); SBAR(); } \
        acc[1] = MFMA32(FBc[1], FAc[1][1], acc[1]); SBAR(); \
        if ((s) + G4_NS < 32) { glds16(bSrc + kpos * 65536, sb_ + 8192 + wid * 1088); kpos = (kpos + 1) & 31; SBAR(); } \
        slot = sn_; } while (0)
#pragma unroll
    for (int s = 0; s < 32; s += 2) {
        G4_STEP(s, fa0, fb0, fa1, fb1);
        G4_STEP(s + 1, fa1, fb1, fa0, fb0);
    }
    STAMP(ws, 4, 2);
#pragma unroll
    for (int mt = 0; mt < 2; ++mt)
#pragma unroll
        for (int gg = 0; gg < 4; ++gg) {
            const unsigned a0 = pr[mt][gg >> 1][0][(gg & 1) * 2], a1 = pr[mt][gg >> 1][0][(gg & 1) * 2 + 1];
            const unsigned b0 = pr[mt][gg >> 1][1][(gg & 1) * 2], b1 = pr[mt][gg >> 1][1][(gg & 1) * 2 + 1];
            const float x0 = __uint_as_float(a0 << 16) + __uint_as_float(b0 << 16);
            const float x1 = __uint_as_float(a0 & 0xffff0000u) + __uint_as_float(b0 & 0xffff0000u);
            const float x2 = __uint_as_float(a1 << 16) + __uint_as_float(b1 << 16);
            const float x3 = __uint_as_float(a1 & 0xffff0000u) + __uint_as_float(b1 & 0xffff0000u);
            *(u32x2*)(out + (size_t)(mb * 128 + wr * 64 + mt * 32 + (lane & 31)) * D_ + nb * 64 + wc * 32 + 8 * gg + 4 * (lane >> 5)) =
                (u32x2){pk_bf16(x0 + acc[mt][4 * gg], x1 + acc[mt][4 * gg + 1]), pk_bf16(x2 + acc[mt][4 * gg + 2], x3 + acc[mt][4 * gg + 3])};
        }
    STAMP(ws, 4, 3);
#undef G4_ISSUE
#undef G4_LOAD
#undef G4_STEP
}

extern "C" void kernel_launch(void* const* d_in, const int* in_sizes, int n_in, void* d_out, int out_size, void* d_ws,
                              size_t ws_size, hipStream_t stream) {
    const u16* x = (const u16*)d_in[0];
    const u16* wqkv = (const u16*)d_in[1];
    const u16* wo = (const u16*)d_in[2];
    const u16* w1 = (const u16*)d_in[4];
    const u16* w2 = (const u16*)d_in[5];
    char* ws = (char*)d_ws;
    (void)hipFuncSetAttribute((const void*)gemm1_kernel, hipFuncAttributeMaxDynamicSharedMemorySize, G1_LDS);
    (void)hipFuncSetAttribute((const void*)k3_kernel, hipFuncAttributeMaxDynamicSharedMemorySize, K3_LDS);
    (void)hipFuncSetAttribute((const void*)k4_kernel, hipFuncAttributeMaxDynamicSharedMemorySize, G4_LDS);
    ln_kernel<<<256, 512, 0, stream>>>(x, (u16*)(ws + WS_XN));
    gemm1_kernel<<<256, 512, G1_LDS, stream>>>((const u16*)(ws + WS_XN), wqkv, w1, ws);
    k3_kernel<<<512, 256, K3_LDS, stream>>>(x, w2, ws);
    k4_kernel<<<256, 256, G4_LDS, stream>>>(x, wo, ws, (u16*)d_out);
}
```

```cpp
#include <hip/hip_runtime.h>
#include <hip/hip_bf16.h>
#include <stdint.h>

typedef unsigned short u16;
using bf16x8 = __attribute__((ext_vector_type(8))) short;
using s16x4  = __attribute__((ext_vector_type(4))) short;
using f32x4  = __attribute__((ext_vector_type(4))) float;
using f32x2  = __attribute__((ext_vector_type(2))) float;
using f32x16 = __attribute__((ext_vector_type(16))) float;
using u32x4  = __attribute__((ext_vector_type(4))) unsigned;
using u32x2  = __attribute__((ext_vector_type(2))) unsigned;
typedef __bf16 bf16x2_t __attribute__((ext_vector_type(2)));
typedef __attribute__((address_space(3))) const char* lds_cptr;
typedef __attribute__((address_space(3))) const bf16x8* lds_b128;
typedef __attribute__((address_space(3))) s16x4* lds_tr;
#define DI __device__ __forceinline__

constexpr int S_ = 2048, D_ = 1024, H_ = 32, DFF = 3584, NQKV = 3072;
constexpr size_t WS_XN = 0, WS_Q = 4u << 20, WS_K = 8u << 20, WS_V = 12u << 20, WS_A2 = 16u << 20, WS_H = 20u << 20, WS_P = 40u << 20;
constexpr int KB2 = 64;

DI unsigned pk_bf16(float lo, float hi) {
    bf16x2_t r = __builtin_convertvector((f32x2){lo, hi}, bf16x2_t);
    return __builtin_bit_cast(unsigned, r);
}
DI float bf2f(u16 v) { return __uint_as_float((unsigned)v << 16); }
DI float rbf(float v) { return __uint_as_float(pk_bf16(v, 0.f) << 16); }

DI void glds16(const void* g, unsigned lds_base) {
    asm volatile("s_mov_b32 m0, %1\n\ts_nop 0\n\tglobal_load_lds_dwordx4 %0, off" :: "v"(g), "s"(lds_base));
}
DI void glds16x2(const void* g, unsigned lds_base) {
    asm volatile("s_mov_b32 m0, %1\n\ts_nop 0\n\tglobal_load_lds_dwordx4 %0, off\n\tglobal_load_lds_dwordx4 %0, off offset:1024" :: "v"(g), "s"(lds_base));
}
DI s16x4 tr_read(lds_cptr p) { return __builtin_amdgcn_ds_read_tr16_b64_v4i16((lds_tr)p); }
DI bf16x8 cat8(s16x4 lo, s16x4 hi) { return __builtin_shufflevector(lo, hi, 0, 1, 2, 3, 4, 5, 6, 7); }
#define MFMA16(a, b, c) __builtin_amdgcn_mfma_f32_16x16x32_bf16((a), (b), (c), 0, 0, 0)
#define MFMA32(a, b, c) __builtin_amdgcn_mfma_f32_32x32x16_bf16((a), (b), (c), 0, 0, 0)
#define WAITBAR(N) do { __builtin_amdgcn_s_waitcnt(0xC07F); asm volatile("s_waitcnt vmcnt(" #N ")\n\ts_barrier" ::: "memory"); } while (0)
#define SBAR() __builtin_amdgcn_sched_barrier(0)

__global__ __launch_bounds__(512) void ln_kernel(const u16* __restrict__ x, u16* __restrict__ xn, const u16* __restrict__ wqkv,
                                                 const u16* __restrict__ w1) {
    __shared__ __attribute__((aligned(16))) u16 tile[8192];
    const int lane = threadIdx.x & 63, wid = threadIdx.x >> 6;
    const int m = blockIdx.x * 8 + wid;
    const u32x4* src = (const u32x4*)(x + (size_t)m * D_);
    const u32x4 raw[2] = {src[lane], src[lane + 64]};
    const int pi = blockIdx.x * 512 + threadIdx.x;
    const u32x4 warm = pi < 61440 ? ((const u32x4*)wqkv)[pi] : ((const u32x4*)w1)[pi - 61440 < 71680 ? pi - 61440 : 0];
    float v[16];
#pragma unroll
    for (int c = 0; c < 2; ++c)
#pragma unroll
        for (int i = 0; i < 4; ++i) {
            v[c * 8 + 2 * i] = __uint_as_float(raw[c][i] << 16);
            v[c * 8 + 2 * i + 1] = __uint_as_float(raw[c][i] & 0xffff0000u);
        }
    float s = 0.f;
#pragma unroll
    for (int i = 0; i < 16; ++i) s += v[i];
#pragma unroll
    for (int o = 1; o < 64; o <<= 1) s += __shfl_xor(s, o);
    const float mean = s * (1.f / D_);
    float q = 0.f;
#pragma unroll
    for (int i = 0; i < 16; ++i) { const float d = v[i] - mean; q += d * d; }
#pragma unroll
    for (int o = 1; o < 64; o <<= 1) q += __shfl_xor(q, o);
    const float mb = rbf(mean), vb = rbf(q * (1.f / D_));
    const float sd = rbf(sqrtf(rbf(vb + 1e-5f)));
#pragma unroll
    for (int c = 0; c < 2; ++c) {
        const int ch = lane + 64 * c;
        const int ks = ch >> 2, cp = ch & 3;
        float o[8];
#pragma unroll
        for (int e = 0; e < 8; ++e) o[e] = rbf(v[c * 8 + e] - mb) / sd;
        const int g0 = (cp >> 1) * 2, j0 = (cp & 1) * 4;
        *(u32x2*)(tile + ((ks * 4 + g0) * 8 + wid) * 8 + j0) = (u32x2){pk_bf16(o[0], o[1]), pk_bf16(o[2], o[3])};
        *(u32x2*)(tile + ((ks * 4 + g0 + 1) * 8 + wid) * 8 + j0) = (u32x2){pk_bf16(o[4], o[5]), pk_bf16(o[6], o[7])};
    }
    __syncthreads();
    const int m0 = blockIdx.x * 8;
#pragma unroll
    for (int i = 0; i < 2; ++i) {
        const int c = threadIdx.x + i * 512, ks = c >> 5, g = (c >> 3) & 3, r = c & 7;
        *(u32x4*)(xn + ((size_t)(((m0 >> 8) * 32 + ks) * 16 + ((m0 >> 4) & 15))) * 512 + (g * 16 + (m0 & 15) + r) * 8) = *(const u32x4*)(tile + c * 8);
    }
    asm volatile("" :: "v"(warm));
}

constexpr int G1_NS = 5, G1_STAGE = 16384 + 13312 + 192, G1_LDS = G1_NS * G1_STAGE;

template <int TN>
DI void gemm1_body(const u16* __restrict__ xn, const u16* __restrict__ wqkv, const u16* __restrict__ w1, char* ws,
                   char* smem, int mb, int nb, int wid, int lane) {
    const int wr = wid & 3, wc = wid >> 2;
    const unsigned lds0 = (unsigned)(uintptr_t)(lds_cptr)smem;
    const char* aSrc = (const char*)xn + (size_t)mb * (32 * 16384) + wid * 2048 + lane * 16;
    const int lr = lane / 26, cc = lane - 26 * lr;
    const int col = nb * 208 + cc * 8;
    const unsigned bstride = col < NQKV ? 2u * NQKV : 2u * DFF;
    const char* bSrc = (col < NQKV ? (const char*)wqkv + col * 2 : (const char*)w1 + (col - NQKV) * 2) + (size_t)(4 * wid + lr) * bstride;
    const bool over = wid == 7 && lr == 2;
    const char* bSrc1 = over ? bSrc : bSrc + 2 * bstride;
    const unsigned binc1 = over ? 0u : 32u * bstride;
    const int g = lane >> 4, q = (lane & 15) >> 2, p = lane & 3;
    const lds_cptr aBase = (lds_cptr)smem + wr * 4096 + lane * 16;
    const lds_cptr bBase = (lds_cptr)smem + 16384 + (16 * (g >> 1) + 4 * (g & 1) + q) * 416 + p * 8 + wc * (7 * 32);

#define G1_PIECE(i, slot) do { const unsigned sb_ = lds0 + (slot) * G1_STAGE; \
        if ((i) == 0) glds16(aSrc, sb_ + wid * 2048); \
        if ((i) == 1) { glds16(aSrc + 1024, sb_ + wid * 2048 + 1024); aSrc += 16384; } \
        if ((i) == 2) { glds16(bSrc, sb_ + 16384 + wid * 1664); bSrc += 32 * bstride; } \
        if ((i) == 3) { glds16(bSrc1, sb_ + 16384 + wid * 1664 + 832); bSrc1 += binc1; } } while (0)
#define G1_ISSUE(slot) do { G1_PIECE(0, slot); G1_PIECE(1, slot); G1_PIECE(2, slot); G1_PIECE(3, slot); } while (0)
#define G1_RD(i, FA, FL, FH, a_, b_) do { if ((i) < 4) FA[(i) & 3] = *(lds_b128)(a_ + ((i) & 3) * 1024); \
        else if ((i) < 4 + 2 * TN) { if (((i) & 1) == 0) FL[(((i) - 4) >> 1) % TN] = tr_read(b_ + (((i) - 4) >> 1) * 32); else FH[(((i) - 4) >> 1) % TN] = tr_read(b_ + 3328 + (((i) - 4) >> 1) * 32); } } while (0)

    f32x4 acc[4][TN];
#pragma unroll
    for (int mt = 0; mt < 4; ++mt)
#pragma unroll
        for (int nt = 0; nt < TN; ++nt) acc[mt][nt] = (f32x4){0.f, 0.f, 0.f, 0.f};
    bf16x8 fa0[4], fa1[4];
    s16x4 fl0[TN], fh0[TN], fl1[TN], fh1[TN];

#pragma unroll
    for (int s = 0; s < G1_NS; ++s) G1_ISSUE(s);
    if (G1_NS == 5) WAITBAR(16); else WAITBAR(12);
#pragma unroll
    for (int i = 0; i < 4 + 2 * TN; ++i) G1_RD(i, fa0, fl0, fh0, aBase, bBase);
    int sl = 0;
#define G1_STEP(FAc, FLc, FHc, FAn, FLn, FHn, W, ISS, LD) do { \
        if ((W) == 3) WAITBAR(12); else if ((W) == 2) WAITBAR(8); else if ((W) == 1) WAITBAR(4); else if ((W) == 0) WAITBAR(0); \
        const int sn_ = sl == G1_NS - 1 ? 0 : sl + 1; \
        const lds_cptr a_ = aBase + sn_ * G1_STAGE; const lds_cptr b_ = bBase + sn_ * G1_STAGE; \
        _Pragma("unroll") for (int nt = 0; nt < TN; ++nt) { \
            const bf16x8 fb_ = cat8(FLc[nt], FHc[nt]); \
            _Pragma("unroll") for (int mt = 0; mt < 4; ++mt) { \
                acc[mt][nt] = MFMA16(fb_, FAc[mt], acc[mt][nt]); \
                if ((LD) && (mt < 3 || TN == 6)) { G1_RD(TN == 7 ? nt * 3 + mt : nt * 4 + mt, FAn, FLn, FHn, a_, b_); } \
                SBAR(); } \
            if ((ISS) && nt >= 1 && nt <= 4) { G1_PIECE(nt - 1, sl); SBAR(); } \
        } \
        sl = sn_; } while (0)
    for (int s = 0; s < 26; s += 2) {
        G1_STEP(fa0, fl0, fh0, fa1, fl1, fh1, G1_NS - 2, true, true);
        G1_STEP(fa1, fl1, fh1, fa0, fl0, fh0, G1_NS - 2, true, true);
    }
    G1_STEP(fa0, fl0, fh0, fa1, fl1, fh1, G1_NS - 2, true, true);
    G1_STEP(fa1, fl1, fh1, fa0, fl0, fh0, G1_NS - 2, G1_NS == 4, true);
    G1_STEP(fa0, fl0, fh0, fa1, fl1, fh1, 2, false, true);
    G1_STEP(fa1, fl1, fh1, fa0, fl0, fh0, 1, false, true);
    G1_STEP(fa0, fl0, fh0, fa1, fl1, fh1, 0, false, true);
    G1_STEP(fa1, fl1, fh1, fa0, fl0, fh0, -1, false, false);
    int lane2 = threadIdx.x; asm volatile("" : "+v"(lane2)); lane2 &= 63;
    const int fr = lane2 & 15, fq = lane2 >> 4;
    const float qscale = 0.17677669529663687f * 1.4426950408889634f;
#pragma unroll
    for (int nt = 0; nt < TN; ++nt) {
        const int N0 = nb * 208 + (wc * 7 + nt) * 16;
        if (N0 >= 2048 && N0 < NQKV) {
            const int hd = (N0 - 2048) >> 5, db = (N0 >> 4) & 1;
#pragma unroll
            for (int mt = 0; mt < 4; ++mt) {
                const int m = mb * 256 + wr * 64 + mt * 16 + fr;
                const f32x4 v = acc[mt][nt];
                *(u32x2*)((u16*)(ws + WS_V) + (size_t)(hd * S_ + m) * 32 + db * 16 + 4 * fq) = (u32x2){pk_bf16(v[0], v[1]), pk_bf16(v[2], v[3])};
            }
        } else if (N0 < 2048) {
            const int hd = (N0 & 1023) >> 5, db = (N0 >> 4) & 1;
            const float sc = N0 < 1024 ? qscale : 1.f;
#pragma unroll
            for (int mp = 0; mp < 2; ++mp) {
                const f32x4 v0 = acc[2 * mp][nt] * sc, v1 = acc[2 * mp + 1][nt] * sc;
                const auto s0 = __builtin_amdgcn_permlane16_swap(pk_bf16(v0[0], v0[1]), pk_bf16(v1[0], v1[1]), false, false);
                const auto s1 = __builtin_amdgcn_permlane16_swap(pk_bf16(v0[2], v0[3]), pk_bf16(v1[2], v1[3]), false, false);
                const int rb = mb * 8 + wr * 2 + mp;
                *(u32x4*)((u16*)(ws + (N0 < 1024 ? WS_Q : WS_K)) + (size_t)((hd * 64 + rb) * 2 + db) * 512 + ((fq & 1) * 16 + fr + 32 * (fq >> 1)) * 8) =
                    (u32x4){s0[0], s1[0], s0[1], s1[1]};
            }
        } else {
            const int kk = N0 - NQKV;
#pragma unroll
            for (int mp = 0; mp < 2; ++mp) {
                f32x4 v0 = acc[2 * mp][nt], v1 = acc[2 * mp + 1][nt];
#pragma unroll
                for (int i = 0; i < 4; ++i) { v0[i] = fmaxf(v0[i], 0.f); v1[i] = fmaxf(v1[i], 0.f); }
                const auto s0 = __builtin_amdgcn_permlane32_swap(pk_bf16(v0[0], v0[1]), pk_bf16(v1[0], v1[1]), false, false);
                const auto s1 = __builtin_amdgcn_permlane32_swap(pk_bf16(v0[2], v0[3]), pk_bf16(v1[2], v1[3]), false, false);
                const int m16 = mb * 16 + wr * 4 + 2 * mp + (fq >> 1);
                *(u32x4*)((u16*)(ws + WS_H) + (size_t)(m16 * 112 + (kk >> 5)) * 512 + (fr + 16 * (2 * ((kk >> 4) & 1) + (fq & 1))) * 8) =
                    (u32x4){s0[0], s1[0], s0[1], s1[1]};
            }
        }
    }
#undef G1_PIECE
#undef G1_ISSUE
#undef G1_RD
#undef G1_STEP
}

__global__ __launch_bounds__(512, 2) void gemm1_kernel(const u16* __restrict__ xn, const u16* __restrict__ wqkv,
                                                       const u16* __restrict__ w1, char* ws) {
    extern __shared__ __attribute__((aligned(16))) char smem[];
    const int wid = __builtin_amdgcn_readfirstlane(threadIdx.x >> 6), lane = threadIdx.x & 63;
    const int xc = blockIdx.x & 7, idx = blockIdx.x >> 3;
    const int mb = (xc & 1) * 4 + (idx & 3), nb = (xc >> 1) * 8 + (idx >> 2);
    if (wid < 4) gemm1_body<7>(xn, wqkv, w1, ws, smem, mb, nb, wid, lane);
    else gemm1_body<6>(xn, wqkv, w1, ws, smem, mb, nb, wid, lane);
}

constexpr int AT_NS = 4, AT_STAGE = 16384, AT_LDS = AT_NS * AT_STAGE;
constexpr float AT_THR = 1.0e36f;

DI void pack_p(const f32x16& s, bf16x8& p0, bf16x8& p1) {
    u32x4 a = {pk_bf16(s[0], s[1]), pk_bf16(s[2], s[3]), pk_bf16(s[4], s[5]), pk_bf16(s[6], s[7])};
    u32x4 b = {pk_bf16(s[8], s[9]), pk_bf16(s[10], s[11]), pk_bf16(s[12], s[13]), pk_bf16(s[14], s[15])};
    p0 = __builtin_bit_cast(bf16x8, a); p1 = __builtin_bit_cast(bf16x8, b);
}
DI float rowmax16(const f32x16& s) {
    float a = fmaxf(fmaxf(s[0], s[1]), s[2]), b = fmaxf(fmaxf(s[3], s[4]), s[5]);
    a = fmaxf(fmaxf(a, s[6]), s[7]); b = fmaxf(fmaxf(b, s[8]), s[9]);
    a = fmaxf(fmaxf(a, s[10]), s[11]); b = fmaxf(fmaxf(b, s[12]), s[13]);
    a = fmaxf(fmaxf(a, s[14]), s[15]);
    a = fmaxf(a, b);
    return fmaxf(a, __shfl_xor(a, 32));
}

DI void attn_slow(char* ws, int head, int lane, bf16x8 q0, bf16x8 q1, bf16x8 ones, f32x16& o, f32x4& l) {
    const bf16x8* kg = (const bf16x8*)(ws + WS_K + (size_t)head * 131072) + lane;
    const u16* vg = (const u16*)(ws + WS_V + (size_t)head * 131072) + (lane & 31) + (lane >> 5) * 128;
    f32x16 zero16;
#pragma unroll
    for (int i = 0; i < 16; ++i) { o[i] = 0.f; zero16[i] = 0.f; }
    l = (f32x4){0.f, 0.f, 0.f, 0.f};
    float m = -3.0e38f;
#pragma unroll 1
    for (int t = 0; t < 64; ++t) {
        f32x16 sr = MFMA32(kg[t * 128], q0, zero16); sr = MFMA32(kg[t * 128 + 64], q1, sr);
        const float mn = fmaxf(rowmax16(sr), m), f = __builtin_amdgcn_exp2f(m - mn);
        m = mn;
#pragma unroll
        for (int i = 0; i < 16; ++i) { o[i] *= f; sr[i] = __builtin_amdgcn_exp2f(sr[i] - mn); }
        l *= f;
        bf16x8 p0, p1, v0, v1; pack_p(sr, p0, p1);
#pragma unroll
        for (int j = 0; j < 8; ++j) {
            v0[j] = (short)vg[(t * 32 + 8 * (j >> 2) + (j & 3)) * 32];
            v1[j] = (short)vg[(t * 32 + 16 + 8 * (j >> 2) + (j & 3)) * 32];
        }
        l = MFMA16(ones, p0, l); l = MFMA16(ones, p1, l);
        o = MFMA32(v0, p0, o); o = MFMA32(v1, p1, o);
    }
}

DI void attn_body(const u16* __restrict__ wo, char* ws, char* smem, int head, int qt, int wid, int lane) {
    const unsigned lds0 = (unsigned)(uintptr_t)(lds_cptr)smem;
    const char* kG = ws + WS_K + (size_t)head * 131072;
    const char* kSrc = kG + wid * 2048 + lane * 16;
    const char* vSrc = ws + WS_V + (size_t)head * 131072 + wid * 2048 + lane * 16;
#define AT_PIECE(i, slot) do { const unsigned sb_ = lds0 + (slot) * AT_STAGE + wid * 2048; \
        if ((i) == 0) glds16(kSrc, sb_); \
        if ((i) == 1) { glds16(kSrc + 1024, sb_ + 1024); kSrc += 8192; } \
        if ((i) == 2) glds16(vSrc, sb_ + 8192); \
        if ((i) == 3) { glds16(vSrc + 1024, sb_ + 9216); vSrc += 8192; } } while (0)
#define AT_ISSUE(slot) do { AT_PIECE(0, slot); AT_PIECE(1, slot); AT_PIECE(2, slot); AT_PIECE(3, slot); } while (0)
    AT_ISSUE(0);
    const int rb = qt * 8 + wid * 2;
    const bf16x8* qp = (const bf16x8*)(ws + WS_Q + (size_t)((head * 64 + rb) * 2) * 1024) + lane;
    const bf16x8 qA0 = qp[0], qA1 = qp[64], qB0 = qp[128], qB1 = qp[192];
    SBAR();
    AT_ISSUE(1);
    const int g = lane >> 4, q = (lane & 15) >> 2, p = lane & 3;
    const lds_cptr kBase = (lds_cptr)smem + lane * 16;
    const lds_cptr vBase = (lds_cptr)smem + 8192 + (4 * (g >> 1) + q) * 64 + (g & 1) * 32 + p * 8;
    const short one = ((g & 1) == ((lane >> 2) & 1)) ? (short)0x3f80 : (short)0;
    const bf16x8 ones = {one, one, one, one, one, one, one, one};
    f32x16 oA, oB, cmA, cmB, sA, sB, zero16;
#pragma unroll
    for (int i = 0; i < 16; ++i) { oA[i] = 0.f; oB[i] = 0.f; zero16[i] = 0.f; }
    f32x4 lA = {0.f, 0.f, 0.f, 0.f}, lB = lA;
    float mA, mB;
    bf16x8 k0, k1, v0, v1;
    u32x4 pA0, pA1, pB0, pB1;
    WAITBAR(4);
    AT_ISSUE(2);
    {
        k0 = *(lds_b128)(kBase); k1 = *(lds_b128)(kBase + 1024);
        sA = MFMA32(k0, qA0, zero16); sB = MFMA32(k0, qB0, zero16);
        sA = MFMA32(k1, qA1, sA); sB = MFMA32(k1, qB1, sB);
        mA = rowmax16(sA); mB = rowmax16(sB);
#pragma unroll
        for (int i = 0; i < 16; ++i) { cmA[i] = -mA; sA[i] -= mA; cmB[i] = -mB; sB[i] -= mB; }
    }
#define BC(x) __builtin_bit_cast(bf16x8, x)
#define EX(S, i) S[i] = __builtin_amdgcn_exp2f(S[i])
#define AT_PHASE(SX, PX0, PX1, SY, PY0, PY1, QY0, QY1, CMY, OY, LY, DOY, DOQK, RDV, voff, RDK, koff, GL, gslot) do { \
        EX(SX, 0); EX(SX, 1); EX(SX, 2); SBAR(); \
        if (DOQK) SY = MFMA32(k0, QY0, CMY); SBAR(); \
        EX(SX, 3); EX(SX, 4); EX(SX, 5); PX0[0] = pk_bf16(SX[0], SX[1]); SBAR(); \
        if (DOY) LY = MFMA16(ones, BC(PY0), LY); SBAR(); \
        EX(SX, 6); EX(SX, 7); EX(SX, 8); PX0[1] = pk_bf16(SX[2], SX[3]); SBAR(); \
        if (DOQK) SY = MFMA32(k1, QY1, SY); SBAR(); \
        if (RDK) { k0 = *(lds_b128)(kBase + (koff)); k1 = *(lds_b128)(kBase + (koff) + 1024); } \
        EX(SX, 9); EX(SX, 10); PX0[2] = pk_bf16(SX[4], SX[5]); PX0[3] = pk_bf16(SX[6], SX[7]); SBAR(); \
        if (DOY) OY = MFMA32(v0, BC(PY0), OY); SBAR(); \
        if ((GL) >= 0) { AT_PIECE(GL, gslot); SBAR(); } \
        EX(SX, 11); EX(SX, 12); EX(SX, 13); PX1[0] = pk_bf16(SX[8], SX[9]); SBAR(); \
        if (DOY) LY = MFMA16(ones, BC(PY1), LY); SBAR(); \
        EX(SX, 14); EX(SX, 15); PX1[1] = pk_bf16(SX[10], SX[11]); PX1[2] = pk_bf16(SX[12], SX[13]); SBAR(); \
        if (DOY) OY = MFMA32(v1, BC(PY1), OY); SBAR(); \
        if (RDV) { v0 = cat8(tr_read(vBase + (voff)), tr_read(vBase + (voff) + 512)); v1 = cat8(tr_read(vBase + (voff) + 1024), tr_read(vBase + (voff) + 1536)); } \
        PX1[3] = pk_bf16(SX[14], SX[15]); SBAR(); } while (0)
#define AT_TILE(t, FIRST, LAST, voff, koff, GL, gslot) do { \
        AT_PHASE(sA, pA0, pA1, sB, pB0, pB1, qB0, qB1, cmB, oB, lB, !(FIRST), !(FIRST), true, voff, !(LAST), koff, GL, gslot); \
        AT_PHASE(sB, pB0, pB1, sA, pA0, pA1, qA0, qA1, cmA, oA, lA, true, !(LAST), false, 0, false, 0, -1, 0); } while (0)

    AT_TILE(0, true, false, 0, 2048, -1, 0);
    AT_TILE(1, false, false, 2048, 4096, -1, 0);
    AT_TILE(2, false, false, 4096, 6144, -1, 0);
    WAITBAR(4);
    AT_TILE(3, false, false, 6144, AT_STAGE, 0, 3);
    for (int st = 1; st < 13; ++st) {
        const int so = (st & 3) * AT_STAGE, sn = ((st + 1) & 3) * AT_STAGE, gp = (st + 2) & 3, gn = (st + 3) & 3;
        AT_TILE(st * 4 + 0, false, false, so, so + 2048, 1, gp);
        AT_TILE(st * 4 + 1, false, false, so + 2048, so + 4096, 2, gp);
        AT_TILE(st * 4 + 2, false, false, so + 4096, so + 6144, 3, gp);
        WAITBAR(4);
        AT_TILE(st * 4 + 3, false, false, so + 6144, sn, 0, gn);
    }
    {
        const int so = AT_STAGE, sn = 2 * AT_STAGE;
        AT_TILE(52, false, false, so, so + 2048, 1, 3);
        AT_TILE(53, false, false, so + 2048, so + 4096, 2, 3);
        AT_TILE(54, false, false, so + 4096, so + 6144, 3, 3);
        WAITBAR(4);
        AT_TILE(55, false, false, so + 6144, sn, -1, 0);
    }
    {   const int so = 2 * AT_STAGE, sn = 3 * AT_STAGE;
        AT_TILE(56, false, false, so, so + 2048, -1, 0);
        AT_TILE(57, false, false, so + 2048, so + 4096, -1, 0);
        AT_TILE(58, false, false, so + 4096, so + 6144, -1, 0);
        WAITBAR(0);
        AT_TILE(59, false, false, so + 6144, sn, -1, 0);
    }
    {   const int so = 3 * AT_STAGE;
        AT_TILE(60, false, false, so, so + 2048, -1, 0);
        AT_TILE(61, false, false, so + 2048, so + 4096, -1, 0);
        AT_TILE(62, false, false, so + 4096, so + 6144, -1, 0);
        AT_TILE(63, false, true, so + 6144, 0, -1, 0);
    }
    const u32x4* wp = (const u32x4*)wo + (head * 8 + qt) * 512 + wid * 128 + lane;
    const u32x4 warm0 = wp[0], warm1 = wp[64];
    lB = MFMA16(ones, BC(pB0), lB); lB = MFMA16(ones, BC(pB1), lB);
    oB = MFMA32(v0, BC(pB0), oB); oB = MFMA32(v1, BC(pB1), oB);
    {
        float chk = fmaxf(lA[0], lB[0]);
#pragma unroll
        for (int i = 0; i < 16; ++i) chk = fmaxf(chk, fmaxf(fabsf(oA[i]), fabsf(oB[i])));
        bool bad = !(chk <= AT_THR) || (lA[0] != lA[0]) || (lB[0] != lB[0]);
#pragma unroll
        for (int i = 0; i < 16; ++i) bad = bad || (oA[i] != oA[i]) || (oB[i] != oB[i]);
        if (__builtin_expect(__any(bad), 0)) {
            attn_slow(ws, head, lane, qA0, qA1, ones, oA, lA);
            attn_slow(ws, head, lane, qB0, qB1, ones, oB, lB);
        }
    }
    const float rA = 1.f / lA[0], rB = 1.f / lB[0];
    u16* ob = (u16*)(ws + WS_A2) + (size_t)(rb * KB2 + 2 * head) * 512 + (lane & 31) * 8 + (lane >> 5) * 4;
#pragma unroll
    for (int gg = 0; gg < 4; ++gg) {
        *(u32x2*)(ob + (gg >> 1) * 512 + (gg & 1) * 256) =
            (u32x2){pk_bf16(oA[4 * gg] * rA, oA[4 * gg + 1] * rA), pk_bf16(oA[4 * gg + 2] * rA, oA[4 * gg + 3] * rA)};
        *(u32x2*)(ob + KB2 * 512 + (gg >> 1) * 512 + (gg & 1) * 256) =
            (u32x2){pk_bf16(oB[4 * gg] * rB, oB[4 * gg + 1] * rB), pk_bf16(oB[4 * gg + 2] * rB, oB[4 * gg + 3] * rB)};
    }
    asm volatile("" :: "v"(warm0), "v"(warm1));
#undef AT_PIECE
#undef AT_ISSUE
#undef EX
#undef BC
#undef AT_PHASE
#undef AT_TILE
}

constexpr int G2_NS = 4, G2_STAGE = 8192 + 8448, G2_LDS = G2_NS * G2_STAGE;

DI void ffn2_body(const u16* __restrict__ x, const u16* __restrict__ w2, char* ws, char* smem, int split, int mb, int nb, int wid, int lane) {
    constexpr int NST = 56;
    const int wr = wid & 1, wc = wid >> 1;
    const unsigned lds0 = (unsigned)(uintptr_t)(lds_cptr)smem;
    const char* aSrc = ws + WS_H + (size_t)((mb * 8 + wid * 2) * 112 + split * NST) * 1024 + lane * 16;
    const char* bSrc = (const char*)w2 + (size_t)(split * 1792 + 2 * wid + 8 * (lane >> 4)) * 2048 + nb * 256 + (lane & 15) * 16;
    const int g = lane >> 4, q = (lane & 15) >> 2, p = lane & 3;
    const lds_cptr aBase = (lds_cptr)smem + wr * 4096 + lane * 16;
    const lds_cptr bBase = (lds_cptr)smem + 8192 + (4 * (g & 1) + q) * 1056 + (g >> 1) * 512 + wc * 128 + p * 8;
#define G2_PIECE(i, slot) do { const unsigned sb_ = lds0 + (slot) * G2_STAGE; \
        if ((i) == 0) glds16(aSrc, sb_ + wid * 2048); \
        if ((i) == 1) { glds16(aSrc + 112 * 1024, sb_ + wid * 2048 + 1024); aSrc += 1024; } \
        if ((i) == 2) glds16(bSrc, sb_ + 8192 + (2 * wid) * 1056); \
        if ((i) == 3) { glds16(bSrc + 2048, sb_ + 8192 + (2 * wid + 1) * 1056); bSrc += 65536; } } while (0)
#define G2_ISSUE(slot) do { G2_PIECE(0, slot); G2_PIECE(1, slot); G2_PIECE(2, slot); G2_PIECE(3, slot); } while (0)
#define G2_RD(i, FA, FL, FH, a_, b_) do { if ((i) < 4) FA[(i) & 3] = *(lds_b128)(a_ + ((i) & 3) * 1024); \
        else if (((i) & 1) == 0) FL[(((i) - 4) >> 1) & 3] = tr_read(b_ + (((i) - 4) >> 1) * 32); else FH[(((i) - 4) >> 1) & 3] = tr_read(b_ + 256 + (((i) - 4) >> 1) * 32); } while (0)
    f32x4 acc[4][4];
#pragma unroll
    for (int mt = 0; mt < 4; ++mt)
#pragma unroll
        for (int nt = 0; nt < 4; ++nt) acc[mt][nt] = (f32x4){0.f, 0.f, 0.f, 0.f};
    bf16x8 fa0[4], fa1[4];
    s16x4 fl0[4], fh0[4], fl1[4], fh1[4];
#pragma unroll
    for (int s = 0; s < G2_NS; ++s) G2_ISSUE(s);
    WAITBAR(12);
#pragma unroll
    for (int i = 0; i < 12; ++i) G2_RD(i, fa0, fl0, fh0, aBase, bBase);
#define G2_STEP(s, FAc, FLc, FHc, FAn, FLn, FHn, W, ISS, LD) do { \
        if ((W) == 8) WAITBAR(8); else if ((W) == 4) WAITBAR(4); else if ((W) == 0) WAITBAR(0); \
        const lds_cptr a_ = aBase + (((s) + 1) & (G2_NS - 1)) * G2_STAGE; const lds_cptr b_ = bBase + (((s) + 1) & (G2_NS - 1)) * G2_STAGE; \
        _Pragma("unroll") for (int nt = 0; nt < 4; ++nt) { \
            const bf16x8 fb_ = cat8(FLc[nt], FHc[nt]); \
            _Pragma("unroll") for (int mt = 0; mt < 4; ++mt) { \
                acc[mt][nt] = MFMA16(fb_, FAc[mt], acc[mt][nt]); \
                if ((LD) && nt < 3) { G2_RD(nt * 4 + mt, FAn, FLn, FHn, a_, b_); } \
                SBAR(); } \
            if ((ISS)) { G2_PIECE(nt, (s) & (G2_NS - 1)); SBAR(); } \
        } } while (0)
    for (int s = 0; s < NST - 4; s += 2) {
        G2_STEP(s, fa0, fl0, fh0, fa1, fl1, fh1, 8, true, true);
        G2_STEP(s + 1, fa1, fl1, fh1, fa0, fl0, fh0, 8, true, true);
    }
    G2_STEP(NST - 4, fa0, fl0, fh0, fa1, fl1, fh1, 8, false, true);
    G2_STEP(NST - 3, fa1, fl1, fh1, fa0, fl0, fh0, 4, false, true);
    G2_STEP(NST - 2, fa0, fl0, fh0, fa1, fl1, fh1, 0, false, true);
    const int fr = lane & 15, fq = lane >> 4;
    u32x2 xv[4][4];
#pragma unroll
    for (int mt = 0; mt < 4; ++mt)
#pragma unroll
        for (int nt = 0; nt < 4; ++nt) xv[mt][nt] = (u32x2){0u, 0u};
    if (split == 0) {
#pragma unroll
        for (int mt = 0; mt < 4; ++mt)
#pragma unroll
            for (int nt = 0; nt < 4; ++nt) xv[mt][nt] = *(const u32x2*)(x + (size_t)(mb * 128 + wr * 64 + mt * 16 + fr) * D_ + nb * 128 + wc * 64 + nt * 16 + 4 * fq);
    }
    SBAR();
    G2_STEP(NST - 1, fa1, fl1, fh1, fa0, fl0, fh0, -1, false, false);
    u16* pb = (u16*)(ws + WS_P) + (size_t)split * (S_ * D_);
#pragma unroll
    for (int mp = 0; mp < 2; ++mp)
#pragma unroll
        for (int nt = 0; nt < 4; ++nt) {
            unsigned w[2][2];
#pragma unroll
            for (int i = 0; i < 2; ++i) {
                const f32x4 v = acc[2 * mp + i][nt]; const u32x2 xx = xv[2 * mp + i][nt];
                w[i][0] = pk_bf16(v[0] + __uint_as_float(xx[0] << 16), v[1] + __uint_as_float(xx[0] & 0xffff0000u));
                w[i][1] = pk_bf16(v[2] + __uint_as_float(xx[1] << 16), v[3] + __uint_as_float(xx[1] & 0xffff0000u));
            }
            const auto s0 = __builtin_amdgcn_permlane32_swap(w[0][0], w[1][0], false, false);
            const auto s1 = __builtin_amdgcn_permlane32_swap(w[0][1], w[1][1], false, false);
            *(u32x4*)(pb + (size_t)((mb * 4 + wr * 2 + mp) * 32 + nb * 4 + wc * 2 + (nt >> 1)) * 1024 + (nt & 1) * 512
                         + ((fq >> 1) * 16 + fr + 32 * (fq & 1)) * 8) = (u32x4){s0[0], s1[0], s0[1], s1[1]};
        }
#undef G2_PIECE
#undef G2_ISSUE
#undef G2_RD
#undef G2_STEP
}

constexpr int K3_LDS = G2_LDS > AT_LDS ? G2_LDS : AT_LDS;
__global__ __launch_bounds__(256, 2) void k3_kernel(const u16* __restrict__ xin, const u16* __restrict__ wo, const u16* __restrict__ w2, char* ws) {
    extern __shared__ __attribute__((aligned(16))) char smem[];
    const int wid = __builtin_amdgcn_readfirstlane(threadIdx.x >> 6), lane = threadIdx.x & 63;
    const int x = blockIdx.x & 7, idx = (blockIdx.x >> 3) & 31;
    if (blockIdx.x < 256) {
        __builtin_amdgcn_s_setprio(2);
        attn_body(wo, ws, smem, x * 4 + (idx >> 3), idx & 7, wid, lane);
    } else {
        ffn2_body(xin, w2, ws, smem, x & 1, ((x >> 1) & 1) * 8 + (idx & 7), (x >> 2) * 4 + (idx >> 3), wid, lane);
    }
}

constexpr int G4_NS = 6, G4_STAGE = 16384 + 8704, G4_LDS = G4_NS * G4_STAGE;

__global__ __launch_bounds__(512, 2) void k4_kernel(const u16* __restrict__ wo, char* ws, u16* __restrict__ out) {
    extern __shared__ __attribute__((aligned(16))) char smem[];
    const int wid = __builtin_amdgcn_readfirstlane(threadIdx.x >> 6), lane = threadIdx.x & 63;
    const int xc = blockIdx.x & 7, idx = blockIdx.x >> 3;
    const int mb = (xc & 3) * 4 + (idx & 3), nb = (xc >> 2) * 8 + (idx >> 2);
    const int wr = wid & 3, wc = wid >> 2;
    const unsigned lds0 = (unsigned)(uintptr_t)(lds_cptr)smem;
    const char* aSrc = ws + WS_A2 + (size_t)((mb * 4 + (wid >> 1)) * KB2 + 2 * (wid & 1)) * 1024 + lane * 16;
    const char* bSrc = (const char*)wo + (size_t)((wid & 3) + 4 * (lane >> 3) + 32 * (wid >> 2)) * 2048 + nb * 128 + (lane & 7) * 16;
    const int g = lane >> 4, q = (lane & 15) >> 2, p = lane & 3;
    const lds_cptr aBase = (lds_cptr)smem + wr * 4096 + lane * 16;
    const lds_cptr bBase = (lds_cptr)smem + 16384 + q * 1088 + (g >> 1) * 256 + wc * 64 + (g & 1) * 32 + p * 8;
    f32x16 acc;
#pragma unroll
    for (int i = 0; i < 16; ++i) acc[i] = 0.f;
#define G4_ISSUE(slot) do { const unsigned sb_ = lds0 + (slot) * G4_STAGE; \
        glds16x2(aSrc, sb_ + wid * 2048); glds16(bSrc, sb_ + 16384 + wid * 1088); aSrc += 4096; bSrc += 131072; } while (0)
#define G4_RD(ks, FA, FB, a_, b_) do { FA[ks] = *(lds_b128)(a_ + (ks) * 1024); \
        FB[ks] = cat8(tr_read(b_ + ((ks) >> 1) * 4352 + ((ks) & 1) * 512), tr_read(b_ + ((ks) >> 1) * 4352 + ((ks) & 1) * 512 + 128)); } while (0)
    bf16x8 fa0[4], fb0[4], fa1[4], fb1[4];
    u32x4 pr[2][2];
    G4_ISSUE(0); G4_ISSUE(1);
    WAITBAR(3);
#pragma unroll
    for (int ks = 0; ks < 4; ++ks) G4_RD(ks, fa0, fb0, aBase, bBase);
#define G4_STEP(s, FAc, FBc, FAn, FBn, I0, I1) do { \
        const lds_cptr a_ = aBase + (((s) + 1) % G4_NS) * G4_STAGE; const lds_cptr b_ = bBase + (((s) + 1) % G4_NS) * G4_STAGE; \
        acc = MFMA32(FBc[0], FAc[0], acc); if ((s) < 15) { G4_RD(0, FAn, FBn, a_, b_); G4_RD(1, FAn, FBn, a_, b_); } SBAR(); \
        acc = MFMA32(FBc[1], FAc[1], acc); if ((s) < 15) { G4_RD(2, FAn, FBn, a_, b_); G4_RD(3, FAn, FBn, a_, b_); } SBAR(); \
        if ((I0) >= 0) { G4_ISSUE((I0) % G4_NS); SBAR(); } \
        acc = MFMA32(FBc[2], FAc[2], acc); SBAR(); \
        if ((I1) >= 0) { G4_ISSUE((I1) % G4_NS); SBAR(); } \
        acc = MFMA32(FBc[3], FAc[3], acc); SBAR(); } while (0)
    WAITBAR(0); G4_STEP(0, fa0, fb0, fa1, fb1, 2, 3);
    WAITBAR(3); G4_STEP(1, fa1, fb1, fa0, fb0, 4, 5);
    WAITBAR(6); G4_STEP(2, fa0, fb0, fa1, fb1, 6, 7);
    WAITBAR(9);
    {
        const u16* pb = (const u16*)(ws + WS_P) + (size_t)((mb * 4 + wr) * 32 + nb * 2 + wc) * 1024 + lane * 8;
        pr[0][0] = *(const u32x4*)(pb); pr[1][0] = *(const u32x4*)(pb + 512);
        pr[0][1] = *(const u32x4*)(pb + (size_t)S_ * D_); pr[1][1] = *(const u32x4*)(pb + (size_t)S_ * D_ + 512);
        SBAR();
    }
    G4_STEP(3, fa1, fb1, fa0, fb0, 8, 9);
    WAITBAR(16); G4_STEP(4, fa0, fb0, fa1, fb1, 10, -1);
    WAITBAR(16); G4_STEP(5, fa1, fb1, fa0, fb0, 11, -1);
    WAITBAR(16); G4_STEP(6, fa0, fb0, fa1, fb1, 12, -1);
    WAITBAR(12); G4_STEP(7, fa1, fb1, fa0, fb0, 13, -1);
    WAITBAR(12); G4_STEP(8, fa0, fb0, fa1, fb1, 14, -1);
    WAITBAR(12); G4_STEP(9, fa1, fb1, fa0, fb0, 15, -1);
    WAITBAR(12); G4_STEP(10, fa0, fb0, fa1, fb1, -1, -1);
    WAITBAR(9); G4_STEP(11, fa1, fb1, fa0, fb0, -1, -1);
    WAITBAR(6); G4_STEP(12, fa0, fb0, fa1, fb1, -1, -1);
    WAITBAR(3); G4_STEP(13, fa1, fb1, fa0, fb0, -1, -1);
    WAITBAR(0); G4_STEP(14, fa0, fb0, fa1, fb1, -1, -1);
    G4_STEP(15, fa1, fb1, fa0, fb0, -1, -1);
    u16* orow = out + (size_t)(mb * 128 + wr * 32 + (lane & 31)) * D_ + nb * 64 + wc * 32 + 8 * (lane >> 5);
#pragma unroll
    for (int u = 0; u < 2; ++u) {
        unsigned w[2][2];
#pragma unroll
        for (int i = 0; i < 2; ++i) {
            const unsigned a0 = pr[u][0][2 * i], a1 = pr[u][0][2 * i + 1], b0 = pr[u][1][2 * i], b1 = pr[u][1][2 * i + 1];
            const int r = 8 * u + 4 * i;
            w[i][0] = pk_bf16(acc[r] + __uint_as_float(a0 << 16) + __uint_as_float(b0 << 16),
                              acc[r + 1] + __uint_as_float(a0 & 0xffff0000u) + __uint_as_float(b0 & 0xffff0000u));
            w[i][1] = pk_bf16(acc[r + 2] + __uint_as_float(a1 << 16) + __uint_as_float(b1 << 16),
                              acc[r + 3] + __uint_as_float(a1 & 0xffff0000u) + __uint_as_float(b1 & 0xffff0000u));
        }
        const auto s0 = __builtin_amdgcn_permlane32_swap(w[0][0], w[1][0], false, false);
        const auto s1 = __builtin_amdgcn_permlane32_swap(w[0][1], w[1][1], false, false);
        *(u32x4*)(orow + 16 * u) = (u32x4){s0[0], s1[0], s0[1], s1[1]};
    }
#undef G4_ISSUE
#undef G4_RD
#undef G4_STEP
}

extern "C" void kernel_launch(void* const* d_in, const int* in_sizes, int n_in, void* d_out, int out_size, void* d_ws,
                              size_t ws_size, hipStream_t stream) {
    const u16* x = (const u16*)d_in[0];
    const u16* wqkv = (const u16*)d_in[1];
    const u16* wo = (const u16*)d_in[2];
    const u16* w1 = (const u16*)d_in[4];
    const u16* w2 = (const u16*)d_in[5];
    char* ws = (char*)d_ws;
    (void)hipFuncSetAttribute((const void*)gemm1_kernel, hipFuncAttributeMaxDynamicSharedMemorySize, G1_LDS);
    (void)hipFuncSetAttribute((const void*)k3_kernel, hipFuncAttributeMaxDynamicSharedMemorySize, K3_LDS);
    (void)hipFuncSetAttribute((const void*)k4_kernel, hipFuncAttributeMaxDynamicSharedMemorySize, G4_LDS);
    ln_kernel<<<256, 512, 0, stream>>>(x, (u16*)(ws + WS_XN), wqkv, w1);
    gemm1_kernel<<<256, 512, G1_LDS, stream>>>((const u16*)(ws + WS_XN), wqkv, w1, ws);
    k3_kernel<<<512, 256, K3_LDS, stream>>>(x, wo, w2, ws);
    k4_kernel<<<256, 512, G4_LDS, stream>>>(wo, ws, (u16*)d_out);
}
```
